```python
import torch

def _softmax(x, axis=-1):
    return torch.softmax(x, dim=axis)
def _gelu(x):
    return torch.nn.functional.gelu(x, approximate="tanh")
def _silu(x):
    return torch.nn.functional.silu(x)
def _sigmoid(x):
    return torch.sigmoid(x)
def _layernorm(x, axis=-1, eps=1e-5):
    m = x.mean(dim=axis, keepdim=True)
    v = x.var(dim=axis, keepdim=True, unbiased=False)
    return (x - m) / torch.sqrt(v + eps)
def _scatter_add(tgt, idx, src, axis=0):
    return tgt.clone().index_add_(axis, idx, src)
def _segment_sum(src, seg, n):
    out = torch.zeros((n,) + src.shape[1:], dtype=src.dtype)
    return out.index_add_(0, seg, src)
def _pad(x, pw):
    flat = tuple(v for pair in reversed(pw) for v in pair)
    return torch.nn.functional.pad(x, flat)

kernel_name = 'recurrent_scan__ssm_conv_s5__mixer_token_mlp__prompt'

def setup_inputs():

    _w = (lambda a: (lambda t: t.to(torch.bfloat16) if t.is_floating_point() else t)(torch.as_tensor(a)))
    def _A_setup():
        B, S, D = 1, 2048, 512
        P = D
        Lambda_re = -0.5 * (lambda _x: torch.exp(torch.as_tensor(_x)))((lambda *s: torch.randn(*s, dtype=torch.bfloat16) * 0.4)(P))
        Lambda_im = (lambda *s: torch.randn(*s, dtype=torch.bfloat16) * 0.8)(P) * 1.0
        B_re = (lambda *s: torch.randn(*s, dtype=torch.bfloat16) * 0.4)(P, D) * (D ** -0.5)
        B_im = (lambda *s: torch.randn(*s, dtype=torch.bfloat16) * 0.4)(P, D) * (D ** -0.5)
        C_re = (lambda *s: torch.randn(*s, dtype=torch.bfloat16) * 0.4)(D, P) * (P ** -0.5)
        C_im = (lambda *s: torch.randn(*s, dtype=torch.bfloat16) * 0.4)(D, P) * (P ** -0.5)
        log_dt = (lambda *s: torch.randn(*s, dtype=torch.bfloat16) * 0.4)(P) * 0.3 - 2.0
        Dskip = (lambda *s: torch.randn(*s, dtype=torch.bfloat16) * 0.2)(D)
        return {
            "x": _w((lambda *s: torch.randn(*s, dtype=torch.bfloat16) * 0.05)(B, S, D)),
            "Lambda_re": _w(Lambda_re),
            "Lambda_im": _w(Lambda_im),
            "B_re": _w(B_re),
            "B_im": _w(B_im),
            "C_re": _w(C_re),
            "C_im": _w(C_im),
            "log_dt": _w(log_dt),
            "Dskip": _w(Dskip),
        }
    _A_in = _A_setup()

    def _B_setup():
        B, S, D, F = 1, 2048, 512, 1376
        return {
            "x": _w((lambda *s: torch.randn(*s, dtype=torch.bfloat16) * 0.05)(B, S, D)),
            "w1": _w((lambda *s: torch.randn(*s, dtype=torch.bfloat16) * 0.4)(S, F) * (S ** -0.5)),
            "b1": _w((lambda *s: torch.randn(*s, dtype=torch.bfloat16) * 3.2)(F) * 0.02),
            "w2": _w((lambda *s: torch.randn(*s, dtype=torch.bfloat16) * 0.8)(F, S) * (F ** -0.5)),
            "b2": _w((lambda *s: torch.randn(*s, dtype=torch.bfloat16) * 1.6)(S) * 0.02),
        }
    _B_in = _B_setup()

    return {
        "A_x": _A_in["x"],
        "A_Lambda_re": _A_in["Lambda_re"],
        "A_Lambda_im": _A_in["Lambda_im"],
        "A_B_re": _A_in["B_re"],
        "A_B_im": _A_in["B_im"],
        "A_C_re": _A_in["C_re"],
        "A_C_im": _A_in["C_im"],
        "A_log_dt": _A_in["log_dt"],
        "A_Dskip": _A_in["Dskip"],
        "B_x": _B_in["x"],
        "B_w1": _B_in["w1"],
        "B_b1": _B_in["b1"],
        "B_w2": _B_in["w2"],
        "B_b2": _B_in["b2"],
    }

def reference(A_x, A_Lambda_re, A_Lambda_im, A_B_re, A_B_im, A_C_re, A_C_im, A_log_dt, A_Dskip, B_x, B_w1, B_b1, B_w2, B_b2):
    def _A_ref(x, Lambda_re, Lambda_im, B_re, B_im, C_re, C_im, log_dt, Dskip):
        Bsz, L, H = x.shape
        Lam = Lambda_re + 1j * Lambda_im
        Bmat = B_re + 1j * B_im
        Cmat = C_re + 1j * C_im
        dt = torch.exp(log_dt)
        Lam_bar = torch.exp(dt * Lam)
        B_bar = ((Lam_bar - 1.0) / Lam)[:, None] * Bmat
        Bu = torch.einsum("bld,pd->blp", x + 0j, B_bar)
        h = torch.zeros_like(Bu[:, 0, :])
        hs = []
        for t in range(L):
            h = Lam_bar[None, :] * h + Bu[:, t, :]
            hs.append(h)
        hseq = torch.stack(hs, 1)
        y = 2.0 * torch.einsum("blp,dp->bld", hseq, Cmat).real + x * Dskip[None, None, :]
        return y

    def _B_ref(x, w1, b1, w2, b2):
        B, S, D = x.shape
        h = _layernorm(x, axis=-1)
        u = torch.einsum("bsd,sf->bdf", h, w1) + b1
        u = _gelu(u)
        y = torch.einsum("bdf,fs->bds", u, w2) + b2
        return (lambda x, *d: x.permute(*d))(y, 0, 2, 1)

    x = A_x
    xn = _layernorm(x, axis=-1)
    s = _A_ref(xn, A_Lambda_re, A_Lambda_im, A_B_re, A_B_im, A_C_re, A_C_im, A_log_dt, A_Dskip)
    g = _silu(_B_ref(xn, B_w1, B_b1, B_w2, B_b2))
    return x + s * g
```

```cpp
#include <hip/hip_runtime.h>
#include <hip/hip_bf16.h>
#include <cstdio>
typedef __hip_bfloat16 bf16;
constexpr int S = 2048, D = 512, P = 512, F = 1376;
__device__ inline float b2f(bf16 v) { return __bfloat162float(v); }

__global__ void n_ln(const bf16* x, float* xn, float* h2) {
    int t = blockIdx.x; int tid = threadIdx.x;
    __shared__ float red[512];
    float v = b2f(x[t * D + tid]);
    red[tid] = v; __syncthreads();
    for (int s = 256; s > 0; s >>= 1) { if (tid < s) red[tid] += red[tid + s]; __syncthreads(); }
    float m = red[0] / D; __syncthreads();
    float c = v - m; red[tid] = c * c; __syncthreads();
    for (int s = 256; s > 0; s >>= 1) { if (tid < s) red[tid] += red[tid + s]; __syncthreads(); }
    float var = red[0] / D; __syncthreads();
    float y = c / sqrtf(var + 1e-5f);
    xn[t * D + tid] = y;
    red[tid] = y; __syncthreads();
    for (int s = 256; s > 0; s >>= 1) { if (tid < s) red[tid] += red[tid + s]; __syncthreads(); }
    float m2 = red[0] / D; __syncthreads();
    float c2 = y - m2; red[tid] = c2 * c2; __syncthreads();
    for (int s = 256; s > 0; s >>= 1) { if (tid < s) red[tid] += red[tid + s]; __syncthreads(); }
    float var2 = red[0] / D; __syncthreads();
    h2[t * D + tid] = c2 / sqrtf(var2 + 1e-5f);
}
__global__ void n_g1(const float* xn, const bf16* Bre, const bf16* Bim, float* bur, float* bui) {
    int t = blockIdx.x; int p = threadIdx.x;
    float ar = 0, ai = 0;
    for (int d = 0; d < D; ++d) { float v = xn[t * D + d]; ar += v * b2f(Bre[p * D + d]); ai += v * b2f(Bim[p * D + d]); }
    bur[t * P + p] = ar; bui[t * P + p] = ai;
}
__global__ void n_scan(const bf16* lre, const bf16* lim, const bf16* ldt, float* bur, float* bui) {
    int p = blockIdx.x * 64 + threadIdx.x;
    double dt = exp((double)b2f(ldt[p])); double lr = b2f(lre[p]), li = b2f(lim[p]);
    double e = exp(dt * lr); double br = e * cos(dt * li), bi = e * sin(dt * li);
    double den = lr * lr + li * li;
    double nr = br - 1.0, ni = bi;
    double cr = (nr * lr + ni * li) / den, ci = (ni * lr - nr * li) / den;
    float fbr = br, fbi = bi, fcr = cr, fci = ci;
    float hr = 0, hi = 0;
    for (int t = 0; t < S; ++t) {
        float ur = bur[t * P + p], ui = bui[t * P + p];
        float vr = fcr * ur - fci * ui, vi = fcr * ui + fci * ur;
        float nhr = fbr * hr - fbi * hi + vr, nhi = fbr * hi + fbi * hr + vi;
        hr = nhr; hi = nhi;
        bur[t * P + p] = hr; bui[t * P + p] = hi;
    }
}
__global__ void n_g3(const float* h2, const bf16* w1, const bf16* b1, float* u) {
    int d = blockIdx.x;
    for (int f = threadIdx.x; f < F; f += blockDim.x) {
        float a = 0;
        for (int s = 0; s < S; ++s) a += h2[s * D + d] * b2f(w1[s * F + f]);
        a += b2f(b1[f]);
        float g = 0.5f * a * (1.f + tanhf(0.7978845608028654f * (a + 0.044715f * a * a * a)));
        u[d * F + f] = g;
    }
}
__global__ void n_fin(const bf16* x, const float* xn, const float* hr, const float* hi, const bf16* Cre, const bf16* Cim,
                      const bf16* Dsk, const float* u, const bf16* w2, const bf16* b2, float* out) {
    int t = blockIdx.x; int d = threadIdx.x;
    float a = 0;
    for (int p = 0; p < P; ++p) a += hr[t * P + p] * b2f(Cre[d * P + p]) - hi[t * P + p] * b2f(Cim[d * P + p]);
    float s = 2.f * a + xn[t * D + d] * b2f(Dsk[d]);
    float y = 0;
    for (int f = 0; f < F; ++f) y += u[d * F + f] * b2f(w2[f * S + t]);
    y += b2f(b2[t]);
    float g = y / (1.f + expf(-y));
    out[t * D + d] = b2f(x[t * D + d]) + s * g;
}
#define NAIVE_NAME kernel_launch
extern "C" void NAIVE_NAME(void* const* d_in, const int* in_sizes, int n_in,
                              void* d_out, int out_size, void* d_ws, size_t ws_size,
                              hipStream_t stream) {
    const bf16* x = (const bf16*)d_in[0];
    float* ws = (float*)d_ws;
    float* xn = ws; float* h2 = xn + S * D; float* bur = h2 + S * D; float* bui = bur + S * P; float* u = bui + S * P;
    n_ln<<<S, 512, 0, stream>>>(x, xn, h2);
    n_g1<<<S, 512, 0, stream>>>(xn, (const bf16*)d_in[3], (const bf16*)d_in[4], bur, bui);
    n_scan<<<P / 64, 64, 0, stream>>>((const bf16*)d_in[1], (const bf16*)d_in[2], (const bf16*)d_in[7], bur, bui);
    n_g3<<<D, 256, 0, stream>>>(h2, (const bf16*)d_in[10], (const bf16*)d_in[11], u);
    n_fin<<<S, 512, 0, stream>>>(x, xn, bur, bui, (const bf16*)d_in[5], (const bf16*)d_in[6], (const bf16*)d_in[8], u,
                                 (const bf16*)d_in[12], (const bf16*)d_in[13], (float*)d_out);
}
```

```cpp
#include <hip/hip_runtime.h>
#include <hip/hip_bf16.h>
#include <stdint.h>

#define LAS __attribute__((address_space(3)))
#define GAS __attribute__((address_space(1)))
#define CAS __attribute__((address_space(4)))
#define DI __device__ __forceinline__
typedef short s16x4 __attribute__((ext_vector_type(4)));
typedef short s16x8 __attribute__((ext_vector_type(8)));
typedef __bf16 bf16x8 __attribute__((ext_vector_type(8)));
typedef __bf16 bf16x2 __attribute__((ext_vector_type(2)));
typedef float f32x2 __attribute__((ext_vector_type(2)));
typedef float f32x4 __attribute__((ext_vector_type(4)));
typedef float f32x16 __attribute__((ext_vector_type(16)));
typedef unsigned u32x2 __attribute__((ext_vector_type(2)));
typedef unsigned u32x4 __attribute__((ext_vector_type(4)));
typedef __amdgpu_buffer_rsrc_t rsrc_t;

constexpr int S = 2048, D = 512, P = 512, F = 1376;
constexpr float EPS = 1e-5f;
constexpr int SC1 = 16;
constexpr int FLS = 128;
constexpr int WS_EPOCH = 0;
constexpr int WS_FL = 4096;
constexpr int WS_FLLN = WS_FL + 256 * FLS;
constexpr int WS_STATS = 65536;
constexpr int WS_E = WS_STATS + S * 16;
constexpr int WS_H2 = 1 << 20;
constexpr int WS_U = WS_H2 + 8 * 128 * 2048;
constexpr int WS_CT = WS_U + 8 * 88 * 2048;
constexpr int WS_H = WS_CT + 8 * 64 * 2048;
constexpr int WS_END = WS_H + 32 * 64 * 2048;
constexpr int WS_STAMP = 32 << 20;
constexpr int RSL = 14;
constexpr int L_RING = 0;
constexpr int L_AUX = 4 * RSL * 2048;
constexpr int LDS_BYTES = 163840;
static_assert(L_AUX + 49152 <= LDS_BYTES, "lds");
constexpr int FIMG = 64 * 272;
static_assert(8 * FIMG <= LDS_BYTES, "lds");
constexpr unsigned SPIN_CAP = 1u << 17;

struct Params {
    const void* x; const void* lre; const void* lim; const void* bre; const void* bim; const void* cre; const void* cim;
    const void* ldt; const void* dsk; const void* w1; const void* b1; const void* w2; const void* b2;
    void* out; void* ws;
};

DI rsrc_t mk(const void* p, int bytes) { return __builtin_amdgcn_make_buffer_rsrc((void*)p, 0, bytes, 0x00020000); }
DI float asf(unsigned v) { return __builtin_bit_cast(float, v); }
DI unsigned asu(float v) { return __builtin_bit_cast(unsigned, v); }
DI bf16x2 asb2(unsigned v) { return __builtin_bit_cast(bf16x2, v); }
DI float bfl(unsigned v) { return __builtin_bit_cast(float, v << 16); }
DI float bfh(unsigned v) { return __builtin_bit_cast(float, v & 0xffff0000u); }
DI unsigned pk(float lo, float hi) { f32x2 v = {lo, hi}; return __builtin_bit_cast(unsigned, __builtin_convertvector(v, bf16x2)); }
DI float ldbf(const void* p, int i) { return bfl(((const GAS unsigned short*)p)[i]); }
DI f32x16 mfma(u32x4 a, u32x4 b, f32x16 c) {
    return __builtin_amdgcn_mfma_f32_32x32x16_bf16(__builtin_bit_cast(bf16x8, a), __builtin_bit_cast(bf16x8, b), c, 0, 0, 0);
}
DI u32x4 trread2(LAS char* lo, LAS char* hi) {
    s16x4 a = __builtin_amdgcn_ds_read_tr16_b64_v4i16((LAS s16x4*)lo);
    s16x4 b = __builtin_amdgcn_ds_read_tr16_b64_v4i16((LAS s16x4*)hi);
    s16x8 r = __builtin_shufflevector(a, b, 0, 1, 2, 3, 4, 5, 6, 7);
    return __builtin_bit_cast(u32x4, r);
}
DI void glds(unsigned ldsaddr, unsigned voff, rsrc_t rs, unsigned soff) {
    asm volatile("s_mov_b32 m0, %0\n\ts_nop 0\n\tbuffer_load_dwordx4 %1, %2, %3 offen lds" ::"s"(ldsaddr), "v"(voff), "s"(rs), "s"(soff) : "memory");
}
template <int N> DI void waitv() { asm volatile("s_waitcnt vmcnt(%0)" ::"i"(N) : "memory"); }
DI void waitl() { asm volatile("s_waitcnt lgkmcnt(0)" ::: "memory"); }
DI void bar() { asm volatile("s_waitcnt lgkmcnt(0)\n\ts_barrier" ::: "memory"); }
#define SCHED() __builtin_amdgcn_sched_barrier(0)
DI float dpp_add(float v) {
    int i;
    i = __builtin_amdgcn_update_dpp(0, __builtin_bit_cast(int, v), 0x111, 0xf, 0xf, true); v += __builtin_bit_cast(float, i);
    i = __builtin_amdgcn_update_dpp(0, __builtin_bit_cast(int, v), 0x112, 0xf, 0xf, true); v += __builtin_bit_cast(float, i);
    i = __builtin_amdgcn_update_dpp(0, __builtin_bit_cast(int, v), 0x114, 0xf, 0xf, true); v += __builtin_bit_cast(float, i);
    i = __builtin_amdgcn_update_dpp(0, __builtin_bit_cast(int, v), 0x118, 0xf, 0xf, true); v += __builtin_bit_cast(float, i);
    i = __builtin_amdgcn_update_dpp(0, __builtin_bit_cast(int, v), 0x142, 0xa, 0xf, false); v += __builtin_bit_cast(float, i);
    i = __builtin_amdgcn_update_dpp(0, __builtin_bit_cast(int, v), 0x143, 0xc, 0xf, false); v += __builtin_bit_cast(float, i);
    return v;
}
DI float wave_sum(float v) { return __builtin_bit_cast(float, __builtin_amdgcn_readlane(__builtin_bit_cast(int, dpp_add(v)), 63)); }
DI void poll_flags(rsrc_t ws, int off, int n, unsigned tlo, unsigned thi, int lane) {
    const int o = off + (lane < n ? lane : 0) * FLS;
    for (unsigned sp = 0; sp < SPIN_CAP; ++sp) {
        asm volatile("" ::: "memory");
        u32x2 v = __builtin_amdgcn_raw_buffer_load_b64(ws, o, 0, SC1);
        if (__builtin_amdgcn_ballot_w64(v.x != tlo || v.y != thi) == 0) break;
        __builtin_amdgcn_s_sleep(1);
    }
    asm volatile("" ::: "memory");
}
DI void set_flag(rsrc_t ws, int off, unsigned tlo, unsigned thi, int lane) {
    if (lane == 0) { u32x2 v = {tlo, thi}; __builtin_amdgcn_raw_buffer_store_b64(v, ws, off, 0, SC1); }
}
DI f32x2 cmul(f32x2 a, f32x2 b) { f32x2 r; r.x = a.x * b.x - a.y * b.y; r.y = a.x * b.y + a.y * b.x; return r; }
DI f32x2 cfma(f32x2 a, f32x2 b, f32x2 c) {
    f32x2 r; r.x = fmaf(a.x, b.x, fmaf(-a.y, b.y, c.x)); r.y = fmaf(a.x, b.y, fmaf(a.y, b.x, c.y)); return r;
}
DI void lam_of(const Params& p, int ch, f32x2& lam, f32x2& Lam) {
    const float dt0 = expf(ldbf(p.ldt, ch));
    const float dt = bfl(pk(dt0, dt0));
    Lam.x = ldbf(p.lre, ch); Lam.y = ldbf(p.lim, ch);
    const float e = expf(dt * Lam.x);
    float sn, cs; sincosf(dt * Lam.y, &sn, &cs);
    lam.x = e * cs; lam.y = e * sn;
}

#define STAMP(k) do { } while (0)

template <int NS, int DA> constexpr int ops_after_w(int i, int at) {
    int n = 0;
    if (i < RSL) { n += 2 * ((RSL < NS ? RSL : NS) - 1 - i); n += 2 * DA; for (int s = 0; s < at; ++s) n += (s + RSL < NS ? 2 : 0) + (s + DA < NS ? 2 : 0); }
    else { const int s0 = i - RSL; n += (s0 + DA < NS ? 2 : 0); for (int s = s0 + 1; s < at; ++s) n += (s + RSL < NS ? 2 : 0) + (s + DA < NS ? 2 : 0); }
    return n > 63 ? 63 : n;
}

DI void ring_fill(unsigned ring, unsigned wv, rsrc_t rw, unsigned ws0, unsigned rowb) {
#pragma unroll 1
    for (int i = 0; i < 2 * RSL; ++i) glds(ring + i * 1024, wv, rw, ws0 + i * 8 * rowb);
}

template <int NS, int DA, bool W_IS_A>
DI void stream_gemm(f32x16 (&acc)[4], LAS char* ring, unsigned ringaddr, unsigned tr0, unsigned tr1, unsigned wv, rsrc_t rw, unsigned ws0,
                    unsigned rowb, rsrc_t ra, unsigned av0, unsigned av1, unsigned as0, bool last_valid) {
    u32x4 af[DA][2], wf[2][2];
#pragma unroll
    for (int i = 0; i < DA; ++i) {
        af[i][0] = __builtin_amdgcn_raw_buffer_load_b128(ra, av0, as0 + i * 2048, SC1);
        af[i][1] = __builtin_amdgcn_raw_buffer_load_b128(ra, av1, as0 + i * 2048, SC1);
    }
    SCHED();
    waitv<ops_after_w<NS, DA>(0, 0)>();
    wf[0][0] = trread2(ring + tr0, ring + tr0 + 1024);
    wf[0][1] = trread2(ring + tr1, ring + tr1 + 1024);
    SCHED();
#pragma unroll
    for (int i = 0; i < NS; ++i) {
        if (i + 1 < NS) {
            const int n = ops_after_w<NS, DA>(i + 1, i);
            asm volatile("s_waitcnt vmcnt(%0)" ::"i"(n) : "memory");
            LAS char* sl = ring + ((i + 1) % RSL) * 2048;
            wf[(i + 1) & 1][0] = trread2(sl + tr0, sl + tr0 + 1024);
            wf[(i + 1) & 1][1] = trread2(sl + tr1, sl + tr1 + 1024);
        }
        SCHED();
        if (i + 1 < NS || last_valid) {
#pragma unroll
            for (int a = 0; a < 4; ++a) {
                if (W_IS_A) acc[a] = mfma(wf[i & 1][a >> 1], af[i % DA][a & 1], acc[a]);
                else        acc[a] = mfma(af[i % DA][a >> 1], wf[i & 1][a & 1], acc[a]);
            }
        }
        SCHED();
        if (i + RSL < NS) {
            glds(ringaddr + (i % RSL) * 2048, wv, rw, ws0 + (i + RSL) * 16 * rowb);
            glds(ringaddr + (i % RSL) * 2048 + 1024, wv, rw, ws0 + ((i + RSL) * 16 + 8) * rowb);
        }
        if (i + DA < NS) {
            af[i % DA][0] = __builtin_amdgcn_raw_buffer_load_b128(ra, av0, as0 + (i + DA) * 2048, SC1);
            af[i % DA][1] = __builtin_amdgcn_raw_buffer_load_b128(ra, av1, as0 + (i + DA) * 2048, SC1);
        }
        SCHED();
    }
}

__global__ void __launch_bounds__(512, 2) fused_kernel(Params p) {
    __shared__ __attribute__((aligned(16))) char smem[LDS_BYTES];
    LAS char* L = (LAS char*)smem;
    const unsigned Lb = (unsigned)(size_t)L;
    const int tid = threadIdx.x, lane = tid & 63, wave = __builtin_amdgcn_readfirstlane(tid >> 6);
    const int b = blockIdx.x, xg = b & 7, j = b >> 3;
    STAMP(0);
    const unsigned long long epoch = *(const CAS unsigned long long*)p.ws + 1ull;
    const unsigned tlo = (unsigned)epoch, thi = (unsigned)(epoch >> 32);
    const rsrc_t rws = mk(p.ws, WS_END);
    const int hh = lane >> 5, l31 = lane & 31, cb = (lane >> 4) & 1, q4 = (lane >> 2) & 3, p4 = lane & 3;
    const unsigned trb = (4 * hh + q4) * 128 + (4 * (q4 >> 1) + 2 * cb + (p4 >> 1)) * 16 + 8 * (p4 & 1);
    const unsigned gch = ((lane & 7) ^ (cb << 2)) * 16;
    const int role = j < 22 ? 0 : (j < 30 ? 1 : 2);
    const int tt = (xg >> 1) * 8 + (j >> 2), dt = (xg & 1) * 4 + (j & 3);
    const rsrc_t rw2 = mk(p.w2, F * S * 2);
    const int g4s0 = wave < 2 ? 22 * wave : 21 * wave + 2;
    const unsigned w2v = (lane >> 3) * (S * 2) + gch, w2s = g4s0 * 16 * (S * 2) + tt * 128;
    LAS char* ring = L + L_RING + (wave & 3) * (RSL * 2048);
    const unsigned ringaddr = Lb + L_RING + (wave & 3) * (RSL * 2048);

    if (role == 0) {
        const int id = (xg >> 1) * 22 + j, nt = id >> 2, mt = (xg & 1) * 4 + (id & 3);
        const bool ln = j < 16;
        const int lg = j * 8 + xg;
        if (wave < 4) {
            const rsrc_t rw1 = mk(p.w1, S * F * 2);
            const unsigned wv = (lane >> 3) * (F * 2) + gch, ws0 = wave * 512 * (F * 2) + nt * 128;
            if (ln) bar();
            ring_fill(ringaddr, wv, rw1, ws0, F * 2);
            asm volatile("" ::"s"(tlo), "s"(thi));
            if (ln) { bar(); bar(); }
            bar();
            STAMP(1);
            f32x16 acc[4];
#pragma unroll
            for (int a = 0; a < 4; ++a) acc[a] = (f32x16)(0.f);
            const unsigned sw = (wave >> 1) * 64, sa = (wave & 1) * 1024;
            stream_gemm<32, 10, true>(acc, ring, ringaddr, trb ^ sw, trb ^ sw ^ 64, wv, rw1, ws0, F * 2, rws,
                                      lane * 16 + sa, lane * 16 + (sa ^ 1024), WS_H2 + (mt * 128 + wave * 32) * 2048, true);
            STAMP(2);
            const rsrc_t rb1 = mk(p.b1, F * 2);
            u32x2 bia[4];
#pragma unroll
            for (int g = 0; g < 4; ++g) bia[g] = __builtin_amdgcn_raw_buffer_load_b64(rb1, (nt * 64 + (wave >> 1) * 32 + 8 * g + 4 * hh) * 2, 0, 0);
#pragma unroll
            for (int a = 1; a < 4; ++a)
#pragma unroll
                for (int g = 0; g < 4; ++g) {
                    f32x4 v = {acc[a][4 * g], acc[a][4 * g + 1], acc[a][4 * g + 2], acc[a][4 * g + 3]};
                    *(LAS f32x4*)(L + L_AUX + ((a ^ wave) * 3 + a - 1) * 4096 + g * 1024 + lane * 16) = v;
                }
            bar();
            u32x4 uf[2];
#pragma unroll
            for (int g = 0; g < 4; ++g) {
                f32x4 v = {acc[0][4 * g], acc[0][4 * g + 1], acc[0][4 * g + 2], acc[0][4 * g + 3]};
#pragma unroll
                for (int a = 0; a < 3; ++a) v += *(LAS f32x4*)(L + L_AUX + (wave * 3 + a) * 4096 + g * 1024 + lane * 16);
                v.x += bfl(bia[g].x); v.y += bfh(bia[g].x); v.z += bfl(bia[g].y); v.w += bfh(bia[g].y);
                float o[4];
#pragma unroll
                for (int e = 0; e < 4; ++e) {
                    const float a = v[e];
                    const float z = a * fmaf(a * a, 0.044715f, 1.0f) * (-2.0f * 0.7978845608028654f * 1.4426950408889634f);
                    o[e] = a * __builtin_amdgcn_rcpf(1.0f + __builtin_amdgcn_exp2f(z));
                }
                uf[g >> 1][(g & 1) * 2] = pk(o[0], o[1]); uf[g >> 1][(g & 1) * 2 + 1] = pk(o[2], o[3]);
            }
            if (nt < 21 || wave < 2) {
                const int uo = WS_U + (mt * 88 + nt * 4 + (wave >> 1) * 2) * 2048 + (wave & 1) * 1024;
                __builtin_amdgcn_raw_buffer_store_b128(uf[0], rws, lane * 16, uo, SC1);
                __builtin_amdgcn_raw_buffer_store_b128(uf[1], rws, lane * 16, uo + 2048, SC1);
            }
            waitv<0>();
            bar();
            if (wave == 0) set_flag(rws, WS_FL + (mt * 22 + nt) * FLS, tlo, thi, lane);
            STAMP(3);
        } else {
            const int v = wave - 4;
            if (ln) {
                u32x4 xr[4];
#pragma unroll
                for (int r = 0; r < 4; ++r) xr[r] = *(const GAS u32x4*)((const GAS char*)p.x + (lg * 16 + 4 * v + r) * 1024 + lane * 16);
                SCHED();
                bar();
                float mean[4], r1[4];
#pragma unroll
                for (int r = 0; r < 4; ++r) {
                    float f[8];
#pragma unroll
                    for (int e = 0; e < 4; ++e) { f[2 * e] = bfl(xr[r][e]); f[2 * e + 1] = bfh(xr[r][e]); }
                    const float m = wave_sum(((f[0] + f[1]) + (f[2] + f[3])) + ((f[4] + f[5]) + (f[6] + f[7]))) * (1.0f / D);
                    float qs = 0.f;
#pragma unroll
                    for (int e = 0; e < 8; ++e) { f[e] -= m; qs = fmaf(f[e], f[e], qs); }
                    const float var = wave_sum(qs) * (1.0f / D);
                    const float ra = __builtin_amdgcn_rsqf(var + EPS);
                    const float rb = __builtin_amdgcn_rsqf(var * ra * ra + EPS);
                    const float sc = ra * rb;
                    u32x4 o;
#pragma unroll
                    for (int e = 0; e < 4; ++e) o[e] = pk(f[2 * e] * sc, f[2 * e + 1] * sc);
                    *(LAS u32x4*)(L + L_AUX + (4 * v + r) * 1024 + ((lane ^ (r << 2)) * 16)) = o;
                    mean[r] = m; r1[r] = ra;
                }
                if (lane < 4) {
                    const float m = lane == 0 ? mean[0] : lane == 1 ? mean[1] : lane == 2 ? mean[2] : mean[3];
                    const float r = lane == 0 ? r1[0] : lane == 1 ? r1[1] : lane == 2 ? r1[2] : r1[3];
                    u32x4 sv = {asu(m), tlo, asu(r), tlo};
                    __builtin_amdgcn_raw_buffer_store_b128(sv, rws, lane * 16, WS_STATS + (lg * 16 + 4 * v) * 16, SC1);
                }
                bar();
#pragma unroll
                for (int i = 0; i < 4; ++i) {
                    LAS char* a = L + L_AUX + (4 * hh + q4) * 1024 + (16 * v + 4 * (i ^ q4) + 2 * cb + (p4 >> 1)) * 16 + 8 * (p4 & 1);
                    const u32x4 fr = trread2(a, a + 8192);
                    __builtin_amdgcn_raw_buffer_store_b128(fr, rws, lane * 16, WS_H2 + ((2 * v + (i >> 1)) * 128 + lg) * 2048 + (i & 1) * 1024, SC1);
                }
                waitv<0>();
                bar();
                if (v == 0) set_flag(rws, WS_FLLN + lg * FLS, tlo, thi, lane);
            }
            STAMP(1);
            poll_flags(rws, WS_FLLN + 32 * v * FLS, 32, tlo, thi, lane);
            bar();
            STAMP(2);
        }
    } else if (role == 1) {
        const int jj = j - 22, ct = (xg >> 1) * 4 + (jj >> 1), pg = (xg & 1) * 2 + (jj & 1);
        const int th = wave >> 2, pw = wave & 3;
        const rsrc_t rx = mk((const char*)p.x + ct * 128 * 1024, 128 * 1024);
        const rsrc_t rb = mk((const char*)((wave & 1) ? p.bim : p.bre) + (pg * 128 + (wave >> 1) * 32) * 1024, 32 * 1024);
        unsigned va[2], vb[4];
#pragma unroll
        for (int i = 0; i < 2; ++i) {
            const int ra = 16 * wave + 8 * i + (lane >> 3), rho = ra & 31;
            const int tok = (ra & 64) + 32 * ((rho >> 2) & 1) + 16 * ((ra >> 5) & 1) + (rho & 3) + 4 * (rho >> 3);
            va[i] = tok * 1024 + (((lane & 7) ^ ((ra >> 1) & 7)) * 16);
        }
#pragma unroll
        for (int i = 0; i < 4; ++i) { const int r = 8 * i + (lane >> 3); vb[i] = r * 1024 + (((lane & 7) ^ ((r >> 1) & 7)) * 16); }
        auto stage = [&](int kt, int buf) {
            const unsigned base = Lb + buf * 49152;
#pragma unroll
            for (int i = 0; i < 2; ++i) glds(base + (2 * wave + i) * 1024, va[i], rx, kt * 128);
#pragma unroll
            for (int i = 0; i < 4; ++i) glds(base + 16384 + (4 * wave + i) * 1024, vb[i], rb, kt * 128);
        };
        stage(0, 0); stage(1, 1); stage(2, 2);
        const int ch = pg * 128 + pw * 32 + l31;
        f32x2 lam, Lam;
        lam_of(p, ch, lam, Lam);
        unsigned rd[4];
#pragma unroll
        for (int ks = 0; ks < 4; ++ks) rd[ks] = l31 * 128 + (((2 * ks + hh) ^ ((lane >> 1) & 7)) * 16);
        f32x16 acc[2][2];
#pragma unroll
        for (int a = 0; a < 4; ++a) acc[a >> 1][a & 1] = (f32x16)(0.f);
        float sb0 = 0.f, sb1 = 0.f;
        const bf16x2 ones = {(__bf16)1.0f, (__bf16)1.0f};
        u32x4 sg;
#pragma unroll
        for (int kt = 0; kt < 8; ++kt) {
            if (kt == 0) waitv<12>(); else if (kt < 7) waitv<6>(); else waitv<0>();
            bar();
            if (kt >= 1 && kt + 2 < 8) stage(kt + 2, (kt + 2) % 3);
            if (kt == 7) sg = __builtin_amdgcn_raw_buffer_load_b128(rws, lane * 16, WS_STATS + (ct * 128 + th * 64) * 16, SC1);
            LAS char* A = L + (kt % 3) * 49152 + th * 8192;
            LAS char* B = L + (kt % 3) * 49152 + 16384 + pw * 8192;
#pragma unroll
            for (int ks = 0; ks < 4; ++ks) {
                const u32x4 a0 = *(LAS u32x4*)(A + rd[ks]), a1 = *(LAS u32x4*)(A + 4096 + rd[ks]);
                const u32x4 b0 = *(LAS u32x4*)(B + rd[ks]), b1 = *(LAS u32x4*)(B + 4096 + rd[ks]);
                acc[0][0] = mfma(a0, b0, acc[0][0]); acc[0][1] = mfma(a0, b1, acc[0][1]);
                acc[1][0] = mfma(a1, b0, acc[1][0]); acc[1][1] = mfma(a1, b1, acc[1][1]);
#pragma unroll
                for (int e = 0; e < 4; ++e) {
                    sb0 = __builtin_amdgcn_fdot2_f32_bf16(asb2(b0[e]), ones, sb0, false);
                    sb1 = __builtin_amdgcn_fdot2_f32_bf16(asb2(b1[e]), ones, sb1, false);
                }
            }
        }
        STAMP(1);
        sb0 += __shfl_xor(sb0, 32); sb1 += __shfl_xor(sb1, 32);
        for (unsigned sp = 0; sp < SPIN_CAP; ++sp) {
            if (__builtin_amdgcn_ballot_w64(sg.y != tlo || sg.w != tlo) == 0) break;
            __builtin_amdgcn_s_sleep(1);
            asm volatile("" ::: "memory");
            sg = __builtin_amdgcn_raw_buffer_load_b128(rws, lane * 16, WS_STATS + (ct * 128 + th * 64) * 16, SC1);
        }
        bar();
        if (wave < 4) ring_fill(ringaddr, w2v, rw2, w2s, S * 2);
        LAS char* X = L + L_AUX;
        {
            const float rs = asf(sg.z);
            f32x2 ab = {rs, -asf(sg.x) * rs};
            *(LAS f32x2*)(X + wave * 512 + lane * 8) = ab;
        }
        waitl();
        f32x2 e = {0.f, 0.f};
#pragma unroll
        for (int i = 0; i < 32; i += 2) {
            const f32x4 ab = *(LAS f32x4*)(X + wave * 512 + hh * 256 + i * 8);
#pragma unroll
            for (int u = 0; u < 2; ++u) {
                const int m = (i + u) >> 4, r = (i + u) & 15;
                f32x2 vv;
                vv.x = fmaf(ab[2 * u], acc[m][0][r], ab[2 * u + 1] * sb0);
                vv.y = fmaf(ab[2 * u], acc[m][1][r], ab[2 * u + 1] * sb1);
                acc[m][0][r] = vv.x; acc[m][1][r] = vv.y;
                e = cfma(lam, e, vv);
            }
        }
        const int sidx = 2 * th + hh, pl = pw * 32 + l31;
        *(LAS f32x2*)(X + 4096 + sidx * 1024 + pl * 8) = e;
        f32x2 l32 = lam;
#pragma unroll
        for (int i = 0; i < 5; ++i) l32 = cmul(l32, l32);
        const f32x2 l64 = cmul(l32, l32), l128 = cmul(l64, l64), l256 = cmul(l128, l128), l512 = cmul(l256, l256);
        bar();
        f32x2 es[4];
#pragma unroll
        for (int s2 = 0; s2 < 4; ++s2) es[s2] = *(LAS f32x2*)(X + 4096 + s2 * 1024 + pl * 8);
        if (sidx == 0) {
            const f32x2 E = cfma(l32, cfma(l32, cfma(l32, es[0], es[1]), es[2]), es[3]);
            u32x4 g = {asu(E.x), tlo, asu(E.y), tlo};
            __builtin_amdgcn_raw_buffer_store_b128(g, rws, ch * 16, WS_E + ct * (P * 16), SC1);
        }
        STAMP(2);
        f32x2 part = {0.f, 0.f};
        {
            u32x4 g[4];
            for (unsigned sp = 0; sp < SPIN_CAP; ++sp) {
                asm volatile("" ::: "memory");
                bool bad = false;
#pragma unroll
                for (int qq = 0; qq < 4; ++qq) {
                    const int jc = ct - 1 - sidx - 4 * qq;
                    g[qq] = __builtin_amdgcn_raw_buffer_load_b128(rws, (jc < 0 ? 0 : jc) * (P * 16) + ch * 16, WS_E, SC1);
                    bad |= jc >= 0 && (g[qq].y != tlo || g[qq].w != tlo);
                }
                if (__builtin_amdgcn_ballot_w64(bad) == 0) break;
                __builtin_amdgcn_s_sleep(1);
            }
#pragma unroll
            for (int qq = 3; qq >= 0; --qq) {
                const int jc = ct - 1 - sidx - 4 * qq;
                f32x2 ev = {jc >= 0 ? asf(g[qq].x) : 0.f, jc >= 0 ? asf(g[qq].z) : 0.f};
                part = cfma(l512, part, ev);
            }
            if (sidx & 1) part = cmul(part, l128);
            if (sidx & 2) part = cmul(part, l256);
        }
        *(LAS f32x2*)(X + 8192 + sidx * 1024 + pl * 8) = part;
        bar();
        STAMP(3);
        f32x2 h = {0.f, 0.f};
#pragma unroll
        for (int s2 = 0; s2 < 4; ++s2) h += *(LAS f32x2*)(X + 8192 + s2 * 1024 + pl * 8);
#pragma unroll
        for (int s2 = 0; s2 < 3; ++s2) if (s2 < sidx) h = cfma(l32, h, es[s2]);
        u32x4 hr[4], hi[4];
#pragma unroll
        for (int i = 0; i < 32; i += 2) {
            const int m = i >> 4, r = i & 15;
            f32x2 v0 = {acc[m][0][r], acc[m][1][r]}, v1 = {acc[m][0][r + 1], acc[m][1][r + 1]};
            const f32x2 h0 = cfma(lam, h, v0);
            h = cfma(lam, h0, v1);
            hr[i >> 3][(i >> 1) & 3] = pk(h0.x, h.x);
            hi[i >> 3][(i >> 1) & 3] = pk(h0.y, h.y);
        }
        LAS char* I = X + 12288 + wave * 4096;
        const int fw = (((l31 >> 1) & 1) << 2) | (l31 & 1) | (((l31 >> 2) & 1) << 1);
        const int ho = WS_H + ((2 * ct + th) * 64 + pg * 16 + pw * 4) * 2048;
#pragma unroll
        for (int part2 = 0; part2 < 2; ++part2) {
#pragma unroll
            for (int c = 0; c < 4; ++c) *(LAS u32x4*)(I + l31 * 128 + (((4 * hh + c) ^ fw) * 16)) = part2 ? hi[c] : hr[c];
            waitl();
            u32x4 fr[4];
#pragma unroll
            for (int kk = 0; kk < 2; ++kk)
#pragma unroll
                for (int ni = 0; ni < 2; ++ni) {
                    const int r0 = 16 * kk + 8 * hh + q4, r1 = r0 + 4;
                    const int f0 = (((r0 >> 1) & 1) << 2) | (r0 & 1) | (((r0 >> 2) & 1) << 1);
                    const int f1 = (((r1 >> 1) & 1) << 2) | (r1 & 1) | (((r1 >> 2) & 1) << 1);
                    const int c = 4 * ni + 2 * cb + (p4 >> 1);
                    fr[kk * 2 + ni] = trread2(I + r0 * 128 + ((c ^ f0) * 16) + 8 * (p4 & 1), I + r1 * 128 + ((c ^ f1) * 16) + 8 * (p4 & 1));
                }
#pragma unroll
            for (int u = 0; u < 4; ++u)
                __builtin_amdgcn_raw_buffer_store_b128(fr[u], rws, lane * 16, ho + (part2 * 2 + (u >> 1)) * 2048 + (u & 1) * 1024, SC1);
            waitl();
        }
        waitv<0>();
        bar();
        if (wave == 0) set_flag(rws, WS_FL + (176 + ct * 4 + pg) * FLS, tlo, thi, lane);
        STAMP(4);
    } else {
        const int cid = xg * 2 + (j - 30);
        if (wave < 4) ring_fill(ringaddr, w2v, rw2, w2s, S * 2);
        u32x4 cr[4], ci[4];
#pragma unroll
        for (int u = 0; u < 4; ++u) {
            const int o = (cid * 32 + l31) * 1024 + (wave * 64 + u * 16 + 8 * hh) * 2;
            cr[u] = *(const GAS u32x4*)((const GAS char*)p.cre + o);
            ci[u] = *(const GAS u32x4*)((const GAS char*)p.cim + o);
        }
        f32x2 lam, Lam;
        lam_of(p, tid, lam, Lam);
        const float inv = 2.0f / (Lam.x * Lam.x + Lam.y * Lam.y);
        f32x2 cf = {((lam.x - 1.0f) * Lam.x + lam.y * Lam.y) * inv, (lam.y * Lam.x - (lam.x - 1.0f) * Lam.y) * inv};
        *(LAS f32x2*)(L + L_AUX + tid * 8) = cf;
        waitl();
#pragma unroll
        for (int u = 0; u < 4; ++u) {
            u32x4 o_re, o_im;
#pragma unroll
            for (int e = 0; e < 4; ++e) {
                const f32x4 k2 = *(LAS f32x4*)(L + L_AUX + (wave * 64 + u * 16 + 8 * hh + 2 * e) * 8);
                const float a0 = bfl(cr[u][e]), b0 = bfl(ci[u][e]), a1 = bfh(cr[u][e]), b1 = bfh(ci[u][e]);
                o_re[e] = pk(a0 * k2.x - b0 * k2.y, a1 * k2.z - b1 * k2.w);
                o_im[e] = pk(-(a0 * k2.y + b0 * k2.x), -(a1 * k2.w + b1 * k2.z));
            }
            const int ks = (wave * 2 + (u >> 1)) * 4 + (u & 1);
            const int o = WS_CT + ((cid >> 1) * 64 + ks) * 2048 + (cid & 1) * 1024;
            __builtin_amdgcn_raw_buffer_store_b128(o_re, rws, lane * 16, o, SC1);
            __builtin_amdgcn_raw_buffer_store_b128(o_im, rws, lane * 16, o + 4096, SC1);
        }
        asm volatile("" ::"s"(tlo), "s"(thi));
        waitv<0>();
        bar();
        if (wave == 0) set_flag(rws, WS_FL + (240 + cid) * FLS, tlo, thi, lane);
        STAMP(1);
    }

    f32x16 acc[4];
#pragma unroll
    for (int a = 0; a < 4; ++a) acc[a] = (f32x16)(0.f);
    if (wave < 4) {
        if (role == 0) ring_fill(ringaddr, w2v, rw2, w2s, S * 2);
        const int n0 = g4s0 >> 2, n1 = (g4s0 + 21 > 85 ? 85 : g4s0 + 21) >> 2;
        poll_flags(rws, WS_FL + (dt * 22 + n0) * FLS, n1 - n0 + 1, tlo, thi, lane);
        STAMP(5);
        stream_gemm<22, 10, false>(acc, ring, ringaddr, trb, trb ^ 64, w2v, rw2, w2s, S * 2, rws,
                                   lane * 16, lane * 16 + 1024, WS_U + (dt * 88 + g4s0) * 2048, wave < 2);
        STAMP(6);
    } else {
        const int q = wave - 4;
        constexpr int CP = 12, HD = 5;
        poll_flags(rws, WS_FL + (240 + 2 * dt) * FLS, 2, tlo, thi, lane);
        STAMP(7);
        u32x4 cf[CP][2], hf[HD][2];
        const int co = WS_CT + (dt * 64 + q * 16) * 2048, ho = WS_H + (tt * 64 + q * 16) * 2048;
#pragma unroll
        for (int i = 0; i < CP; ++i) {
            cf[i][0] = __builtin_amdgcn_raw_buffer_load_b128(rws, lane * 16, co + i * 2048, SC1);
            cf[i][1] = __builtin_amdgcn_raw_buffer_load_b128(rws, lane * 16 + 1024, co + i * 2048, SC1);
        }
        SCHED();
        if (role == 0) { bar(); bar(); }
        poll_flags(rws, WS_FL + (176 + (tt >> 1) * 4 + q) * FLS, 1, tlo, thi, lane);
        STAMP(5);
#pragma unroll
        for (int i = 0; i < HD; ++i) {
            hf[i][0] = __builtin_amdgcn_raw_buffer_load_b128(rws, lane * 16, ho + i * 2048, SC1);
            hf[i][1] = __builtin_amdgcn_raw_buffer_load_b128(rws, lane * 16 + 1024, ho + i * 2048, SC1);
        }
        SCHED();
#pragma unroll
        for (int i = 0; i < 16; ++i) {
#pragma unroll
            for (int a = 0; a < 4; ++a) acc[a] = mfma(cf[i % CP][a >> 1], hf[i % HD][a & 1], acc[a]);
            SCHED();
            if (i + CP < 16) {
                cf[i % CP][0] = __builtin_amdgcn_raw_buffer_load_b128(rws, lane * 16, co + (i + CP) * 2048, SC1);
                cf[i % CP][1] = __builtin_amdgcn_raw_buffer_load_b128(rws, lane * 16 + 1024, co + (i + CP) * 2048, SC1);
            }
            if (i + HD < 16) {
                hf[i % HD][0] = __builtin_amdgcn_raw_buffer_load_b128(rws, lane * 16, ho + (i + HD) * 2048, SC1);
                hf[i % HD][1] = __builtin_amdgcn_raw_buffer_load_b128(rws, lane * 16 + 1024, ho + (i + HD) * 2048, SC1);
            }
            SCHED();
        }
        STAMP(6);
    }
    const int et = tt * 64 + (tid >> 3), ed = dt * 64 + (tid & 7) * 4;
    const u32x2 x0 = *(const GAS u32x2*)((const GAS char*)p.x + (et * D + ed) * 2);
    const u32x2 x1 = *(const GAS u32x2*)((const GAS char*)p.x + (et * D + ed + 32) * 2);
    const u32x2 k0 = *(const GAS u32x2*)((const GAS char*)p.dsk + ed * 2);
    const u32x2 k1 = *(const GAS u32x2*)((const GAS char*)p.dsk + (ed + 32) * 2);
    const float bb2 = ldbf(p.b2, et);
    const u32x4 sg = __builtin_amdgcn_raw_buffer_load_b128(rws, et * 16, WS_STATS, SC1);
    bar();
#pragma unroll
    for (int a = 0; a < 4; ++a)
#pragma unroll
        for (int g = 0; g < 4; ++g) {
            f32x4 v = {acc[a][4 * g], acc[a][4 * g + 1], acc[a][4 * g + 2], acc[a][4 * g + 3]};
            *(LAS f32x4*)(L + wave * FIMG + ((a & 1) * 32 + l31) * 272 + ((a >> 1) * 32 + 8 * g + 4 * hh) * 4) = v;
        }
    bar();
    STAMP(8);
    const float mean = asf(sg.x), rstd = asf(sg.z);
#pragma unroll
    for (int hf2 = 0; hf2 < 2; ++hf2) {
        LAS char* R = L + (tid >> 3) * 272 + (tid & 7) * 16 + hf2 * 128;
        f32x4 y2 = *(LAS f32x4*)(R), y = *(LAS f32x4*)(R + 4 * FIMG);
#pragma unroll
        for (int w = 1; w < 4; ++w) { y2 += *(LAS f32x4*)(R + w * FIMG); y += *(LAS f32x4*)(R + (4 + w) * FIMG); }
        const u32x2 xx = hf2 ? x1 : x0, kk = hf2 ? k1 : k0;
        const float xf[4] = {bfl(xx.x), bfh(xx.x), bfl(xx.y), bfh(xx.y)};
        const float kf[4] = {bfl(kk.x), bfh(kk.x), bfl(kk.y), bfh(kk.y)};
        f32x4 o;
#pragma unroll
        for (int e = 0; e < 4; ++e) {
            const float z = y2[e] + bb2;
            const float g = z * __builtin_amdgcn_rcpf(1.0f + __builtin_amdgcn_exp2f(z * -1.4426950408889634f));
            const float s = fmaf((xf[e] - mean) * rstd, kf[e], y[e]);
            o[e] = fmaf(s, g, xf[e]);
        }
        *(GAS f32x4*)((GAS char*)p.out + (et * D + ed + hf2 * 32) * 4) = o;
    }
    STAMP(9);
    if (b == 0 && wave == 7) {
#pragma unroll 1
        for (int i = 0; i < 4; ++i) poll_flags(rws, WS_FL + 64 * i * FLS, 64, tlo, thi, lane);
        set_flag(rws, WS_EPOCH, tlo, thi, lane);
    }
}

extern "C" void kernel_launch(void* const* d_in, const int* in_sizes, int n_in, void* d_out, int out_size, void* d_ws, size_t ws_size,
                              hipStream_t stream) {
    Params p;
    p.x = d_in[0]; p.lre = d_in[1]; p.lim = d_in[2]; p.bre = d_in[3]; p.bim = d_in[4]; p.cre = d_in[5]; p.cim = d_in[6];
    p.ldt = d_in[7]; p.dsk = d_in[8]; p.w1 = d_in[10]; p.b1 = d_in[11]; p.w2 = d_in[12]; p.b2 = d_in[13];
    p.out = d_out; p.ws = d_ws;
    fused_kernel<<<256, 512, 0, stream>>>(p);
}
```

```cpp
#include <hip/hip_runtime.h>
#include <hip/hip_bf16.h>
#include <stdint.h>
#include <utility>

#define LAS __attribute__((address_space(3)))
#define GAS __attribute__((address_space(1)))
#define CAS __attribute__((address_space(4)))
#define DI __device__ __forceinline__
typedef short s16x4 __attribute__((ext_vector_type(4)));
typedef short s16x8 __attribute__((ext_vector_type(8)));
typedef __bf16 bf16x8 __attribute__((ext_vector_type(8)));
typedef __bf16 bf16x2 __attribute__((ext_vector_type(2)));
typedef float f32x2 __attribute__((ext_vector_type(2)));
typedef float f32x4 __attribute__((ext_vector_type(4)));
typedef float f32x16 __attribute__((ext_vector_type(16)));
typedef unsigned u32x2 __attribute__((ext_vector_type(2)));
typedef unsigned u32x4 __attribute__((ext_vector_type(4)));
typedef __amdgpu_buffer_rsrc_t rsrc_t;

constexpr int S = 2048, D = 512, P = 512, F = 1376;
constexpr float EPS = 1e-5f;
constexpr int SC1 = 16;
constexpr int FLS = 128;
constexpr int WS_EPOCH = 0;
constexpr int WS_FLU = 4096;
constexpr int WS_FLH = WS_FLU + 176 * FLS;
constexpr int WS_FLC = WS_FLH + 64 * FLS;
constexpr int WS_FLLN = WS_FLC + 16 * FLS;
constexpr int WS_STATS = 256 * 1024;
constexpr int WS_CH = WS_STATS + S * 16;
constexpr int WS_E = WS_CH + P * 32;
constexpr int WS_H2 = 1 << 20;
constexpr int WS_U = WS_H2 + 8 * 128 * 2048;
constexpr int WS_CT = WS_U + 8 * 88 * 2048;
constexpr int WS_H = WS_CT + 8 * 64 * 2048;
constexpr int WS_PX = WS_H + 32 * 64 * 2048;
constexpr int WS_END = WS_PX + 44 * 16 * 9216;
constexpr int WS_STAMP = 32 << 20;
static_assert(WS_FLLN + 512 * FLS <= WS_STATS && WS_E + 16 * P * 16 <= WS_H2 && WS_END <= WS_STAMP, "ws");
constexpr int RSL = 11;
constexpr int L_RING = 0;
constexpr int L_AUX = 4 * RSL * 2048;
constexpr int L_LN = 131072;
constexpr int L_DUMMY = L_AUX + 65536;
constexpr int L_CNT = L_DUMMY + 4096;
constexpr int LDS_BYTES = 163840;
static_assert(L_CNT + 64 <= LDS_BYTES && 3 * 49152 <= L_DUMMY && L_LN + 16384 <= L_DUMMY, "lds");
constexpr int C_SEEN = 0, C_LN = 4, C_LNST = 8, C_G3 = 12, C_RING = 16, C_PUB = 20, C_PUB2 = 24, C_GO = 28, C_XL = 32, C_H1 = 36, C_H2 = 40;
constexpr unsigned SPIN_CAP = 1u << 17;

struct Params {
    const void* x; const void* lre; const void* lim; const void* bre; const void* bim; const void* cre; const void* cim;
    const void* ldt; const void* dsk; const void* w1; const void* b1; const void* w2; const void* b2;
    void* out; void* ws;
};

DI rsrc_t mk(const void* p, int bytes) { return __builtin_amdgcn_make_buffer_rsrc((void*)p, 0, bytes, 0x00020000); }
DI float asf(unsigned v) { return __builtin_bit_cast(float, v); }
DI unsigned asu(float v) { return __builtin_bit_cast(unsigned, v); }
DI bf16x2 asb2(unsigned v) { return __builtin_bit_cast(bf16x2, v); }
DI float bfl(unsigned v) { return __builtin_bit_cast(float, v << 16); }
DI float bfh(unsigned v) { return __builtin_bit_cast(float, v & 0xffff0000u); }
DI unsigned pk(float lo, float hi) { f32x2 v = {lo, hi}; return __builtin_bit_cast(unsigned, __builtin_convertvector(v, bf16x2)); }
DI float ldbf(const void* p, int i) { return bfl(((const GAS unsigned short*)p)[i]); }
DI f32x16 mfma(u32x4 a, u32x4 b, f32x16 c) {
    return __builtin_amdgcn_mfma_f32_32x32x16_bf16(__builtin_bit_cast(bf16x8, a), __builtin_bit_cast(bf16x8, b), c, 0, 0, 0);
}
DI u32x4 trread2(LAS char* lo, LAS char* hi) {
    s16x4 a = __builtin_amdgcn_ds_read_tr16_b64_v4i16((LAS s16x4*)lo);
    s16x4 b = __builtin_amdgcn_ds_read_tr16_b64_v4i16((LAS s16x4*)hi);
    s16x8 r = __builtin_shufflevector(a, b, 0, 1, 2, 3, 4, 5, 6, 7);
    return __builtin_bit_cast(u32x4, r);
}
template <bool NTL> DI void glds(unsigned ldsaddr, unsigned voff, rsrc_t rs, unsigned soff) {
    if (NTL) asm volatile("s_mov_b32 m0, %0\n\ts_nop 0\n\tbuffer_load_dwordx4 %1, %2, %3 offen nt lds" ::"s"(ldsaddr), "v"(voff), "s"(rs), "s"(soff) : "memory");
    else     asm volatile("s_mov_b32 m0, %0\n\ts_nop 0\n\tbuffer_load_dwordx4 %1, %2, %3 offen lds" ::"s"(ldsaddr), "v"(voff), "s"(rs), "s"(soff) : "memory");
}
template <int N> DI void waitv() { asm volatile("s_waitcnt vmcnt(%0)" ::"i"(N) : "memory"); }
DI void waitl() { asm volatile("s_waitcnt lgkmcnt(0)" ::: "memory"); }
DI void bar() { asm volatile("s_waitcnt lgkmcnt(0)\n\ts_barrier" ::: "memory"); }
#define SCHED() __builtin_amdgcn_sched_barrier(0)
template <int CTRL> DI float dpp(float v) { return asf((unsigned)__builtin_amdgcn_update_dpp(0, (int)asu(v), CTRL, 0xf, 0xf, true)); }
DI float row_sum(float v) {
    v += dpp<0x128>(v); v += dpp<0x124>(v); v += dpp<0x122>(v); v += dpp<0x121>(v);
    return v;
}
DI float other_half(float v) { return __shfl_xor(v, 32); }
DI void poll_flags(rsrc_t ws, int o, unsigned tlo, unsigned thi) {
#pragma unroll 1
    for (unsigned sp = 0; sp < SPIN_CAP; ++sp) {
        asm volatile("" ::: "memory");
        u32x2 v = __builtin_amdgcn_raw_buffer_load_b64(ws, o, 0, SC1);
        if (__builtin_amdgcn_ballot_w64(v.x != tlo || v.y != thi) == 0) break;
        __builtin_amdgcn_s_sleep(1);
    }
    asm volatile("" ::: "memory");
}
DI void lds_add(LAS char* cnt, int lane) {
    asm volatile("ds_add_u32 %0, %1" ::"v"((unsigned)(size_t)cnt + (lane ? 2044 + lane * 4 : 0)), "v"(1u) : "memory");
}
DI void lds_arrive(LAS char* cnt, int lane) { waitl(); lds_add(cnt, lane); }
template <int SLEEP> DI void lds_wait(LAS char* cnt, unsigned n) {
#pragma unroll 1
    for (unsigned sp = 0; sp < (SPIN_CAP << 4); ++sp) {
        asm volatile("" ::: "memory");
        if ((unsigned)__builtin_amdgcn_readfirstlane(*(volatile LAS unsigned*)cnt) >= n) break;
        if (SLEEP) __builtin_amdgcn_s_sleep(SLEEP);
    }
    asm volatile("" ::: "memory");
}
DI bool lds_last(LAS char* cnt, unsigned n, int lane) {
    unsigned old = 0;
    if (lane == 0) old = __hip_atomic_fetch_add((LAS unsigned*)cnt, 1u, __ATOMIC_RELAXED, __HIP_MEMORY_SCOPE_WORKGROUP);
    asm volatile("" ::: "memory");
    return (unsigned)__builtin_amdgcn_readfirstlane(old) == n - 1;
}
DI void set_flag(rsrc_t ws, int off, unsigned tlo, unsigned thi, int lane) {
    if (lane == 0) { u32x2 v = {tlo, thi}; __builtin_amdgcn_raw_buffer_store_b64(v, ws, off, 0, SC1); }
}
DI f32x2 cmul(f32x2 a, f32x2 b) { f32x2 r; r.x = a.x * b.x - a.y * b.y; r.y = a.x * b.y + a.y * b.x; return r; }
DI f32x2 cfma(f32x2 a, f32x2 b, f32x2 c) {
    f32x2 r; r.x = fmaf(a.x, b.x, fmaf(-a.y, b.y, c.x)); r.y = fmaf(a.x, b.y, fmaf(a.y, b.x, c.y)); return r;
}
DI void lam_of(const Params& p, int ch, f32x2& lam, f32x2& Lam) {
    const float dt0 = expf(ldbf(p.ldt, ch));
    const float dt = bfl(pk(dt0, dt0));
    Lam.x = ldbf(p.lre, ch); Lam.y = ldbf(p.lim, ch);
    const float e = expf(dt * Lam.x);
    float sn, cs; sincosf(dt * Lam.y, &sn, &cs);
    lam.x = e * cs; lam.y = e * sn;
}

#define STAMP(k) do { } while (0)
#define CYC(v) do { } while (0)

#define W2NT false
#define STHR 5
#define THR 6
#define CODE_LEN 32256
template <class Fn, int... I> DI void sfor_impl(Fn&& f, std::integer_sequence<int, I...>) { (f(std::integral_constant<int, I>{}), ...); }
template <int N, class Fn> DI void sfor(Fn&& f) { sfor_impl(f, std::make_integer_sequence<int, N>{}); }
constexpr int tile_of(int n) { return n < 64 ? 3 - (n & 1) : (n < 96 ? 1 : 0); }
constexpr int step_of(int n) { return n < 64 ? n >> 1 : (n & 31); }
struct Walk {
    unsigned cur, lo, hi, step;
    DI Walk(unsigned base, unsigned st, int n, int rot) : cur(base + rot * st), lo(base), hi(base + n * st), step(st) {}
    DI unsigned next() { const unsigned r = cur; cur += step; cur = cur == hi ? lo : cur; return r; }
};
DI void w_issue(unsigned slotaddr, unsigned wv, rsrc_t rw, Walk& w) {
    const unsigned o = w.next();
    glds<W2NT>(slotaddr, wv, rw, o);
    glds<W2NT>(slotaddr + 1024, wv, rw, o + 8 * S * 2);
}
struct Epi { u32x2 x0, x1, k0, k1; u32x4 sg; float b2; };
DI void epi_load(Epi& e, const Params& p, rsrc_t rws, int tt, int dt, int tid) {
    const int et = tt * 64 + (tid >> 3), ed = dt * 64 + (tid & 7) * 4;
    e.x0 = *(const GAS u32x2*)((const GAS char*)p.x + (et * D + ed) * 2);
    e.x1 = *(const GAS u32x2*)((const GAS char*)p.x + (et * D + ed + 32) * 2);
    e.k0 = *(const GAS u32x2*)((const GAS char*)p.dsk + ed * 2);
    e.k1 = *(const GAS u32x2*)((const GAS char*)p.dsk + (ed + 32) * 2);
    e.b2 = ldbf(p.b2, et);
    e.sg = __builtin_amdgcn_raw_buffer_load_b128(rws, et * 16, WS_STATS, SC1);
}

__global__ void __launch_bounds__(512, 2) fused_kernel(Params p) {
    __shared__ __attribute__((aligned(16))) char smem[LDS_BYTES];
    LAS char* L = (LAS char*)smem;
    const unsigned Lb = (unsigned)(size_t)L;
    const int b = blockIdx.x, xg = b & 7, j = b >> 3;
    const int role = j >= 24 ? 1 : (((xg & 1) && j >= 20) ? 2 : 0);
    const int tid = threadIdx.x ^ (role == 1 ? 0 : 256), lane = tid & 63, wave = __builtin_amdgcn_readfirstlane(tid >> 6);
    STAMP(0);
    {
        unsigned long long pc;
        constexpr unsigned len = CODE_LEN;
        asm volatile("s_getpc_b64 %0" : "=s"(pc));
        const rsrc_t rc = mk((const void*)(pc & ~255ull), len);
        const unsigned piece = ((j - 16) * 8 + wave) * 1024;
        if (piece < len) glds<false>(Lb + L_DUMMY + (wave & 3) * 1024, lane * 16, rc, piece);
    }
    u32x4 xr[4];
    if (j < 16 && wave >= 4) {
#pragma unroll
        for (int c = 0; c < 4; ++c)
            xr[c] = __builtin_nontemporal_load((const GAS u32x4*)((const GAS char*)p.x + (xg * 256 + j * 16 + 4 * (wave - 4) + (lane >> 4)) * 1024 + ((lane & 15) + 16 * c) * 16));
    }
    const rsrc_t rws = mk(p.ws, WS_END);
    const u32x2 eraw = __builtin_amdgcn_raw_buffer_load_b64(rws, WS_EPOCH, 0, SC1);
    if (threadIdx.x < 16) *(LAS unsigned*)(L + L_CNT + threadIdx.x * 4) = 0u;
    bar();
    STAMP(12);
    if (role == 0 && wave < 4) {
        const rsrc_t rw1 = mk(p.w1, S * F * 2);
        const unsigned wv = (lane >> 4) * (F * 2) + ((lane & 15) ^ ((lane >> 4) << 2)) * 16;
        const unsigned wb = ((xg >> 1) * 512 + wave * 4) * (F * 2) + ((xg & 1) * 6 + (j >> 2)) * 256;
#pragma unroll 1
        for (int i = 0; i < 32; ++i) {
            const int ks = (i + 8 * j) & 31;
            glds<true>(Lb + ks * 4096 + wave * 1024, wv, rw1, wb + ks * 16 * (F * 2));
            waitv<STHR>();
        }
    }
    if (j == 16 && wave >= 4) {
        const rsrc_t rv = wave == 4 ? mk(p.b1, F * 2) : (wave == 5 ? mk(p.dsk, D * 2) : mk((const char*)p.b2 + xg * 512, 512));
#pragma unroll 1
        for (int i = 0; i < 3; ++i) glds<false>(Lb + L_DUMMY + (wave & 3) * 1024, lane * 16, rv, i * 1024);
    }
    unsigned tlo, thi;
#define GET_TAG()                                                                     \
    tlo = __builtin_amdgcn_readfirstlane(eraw.x) + 1u;                                \
    thi = __builtin_amdgcn_readfirstlane(eraw.y);                                     \
    asm volatile("" ::"s"(tlo), "s"(thi));                                            \
    lds_add(L + L_CNT + C_SEEN, lane)
#define TLO tlo
#define THI thi
    const int hh = lane >> 5, l31 = lane & 31, cb = (lane >> 4) & 1, q4 = (lane >> 2) & 3, p4 = lane & 3;
    const int tt = xg * 4 + (j >> 3), dt = j & 7;
    const int ct = xg * 2 + ((j >> 2) & 1), pg = j & 3;
    const rsrc_t rw2 = mk(p.w2, F * S * 2);
    const int wq = wave & 3;
    const int s0 = wq < 2 ? 22 * wq : 21 * wq + 2, ns = wq < 2 ? 22 : 21;
    const int rot = (dt * 21) >> 3;
    const unsigned w2v = (lane >> 3) * (S * 2) + ((lane & 7) ^ (cb << 2)) * 16;
    const unsigned ringaddr = Lb + L_RING + wq * (RSL * 2048);
    f32x16 acc[4];
    Epi ep;

    if (role == 2) {
        const int cid = (xg >> 1) * 4 + (j - 20), g = lane >> 4, li = lane & 15, p0 = wave * 64;
        u32x4 cr[4], ci[4], br[2][4];
#pragma unroll
        for (int u = 0; u < 4; ++u) {
            const int o = (cid * 32 + l31) * 1024 + (p0 + u * 16 + 8 * hh) * 2;
            cr[u] = *(const GAS u32x4*)((const GAS char*)p.cre + o);
            ci[u] = *(const GAS u32x4*)((const GAS char*)p.cim + o);
        }
        f32x2 lam, Lam;
        lam_of(p, p0 + lane, lam, Lam);
        GET_TAG();
        const int brow = cid * 32 + wave * 4 + g;
#pragma unroll
        for (int c = 0; c < 8; ++c)
            br[c >> 2][c & 3] = *(const GAS u32x4*)((const GAS char*)(c < 4 ? p.bre : p.bim) + brow * 1024 + (li + 16 * (c & 3)) * 16);
        const float inv = 2.0f / (Lam.x * Lam.x + Lam.y * Lam.y);
        f32x2 cf = {((lam.x - 1.0f) * Lam.x + lam.y * Lam.y) * inv, (lam.y * Lam.x - (lam.x - 1.0f) * Lam.y) * inv};
        LAS char* K = L + (wave < 4 ? L_RING + wave * (RSL * 2048) : L_AUX + (wave - 4) * 16384);
        *(LAS f32x2*)(K + lane * 8) = cf;
        waitl();
#pragma unroll
        for (int u = 0; u < 4; ++u) {
            u32x4 o_re, o_im;
#pragma unroll
            for (int e = 0; e < 4; ++e) {
                const f32x4 k2 = *(LAS f32x4*)(K + (u * 16 + 8 * hh + 2 * e) * 8);
                const float a0 = bfl(cr[u][e]), b0 = bfl(ci[u][e]), a1 = bfh(cr[u][e]), b1 = bfh(ci[u][e]);
                o_re[e] = pk(a0 * k2.x - b0 * k2.y, a1 * k2.z - b1 * k2.w);
                o_im[e] = pk(-(a0 * k2.y + b0 * k2.x), -(a1 * k2.w + b1 * k2.z));
            }
            const int o = WS_CT + ((cid >> 1) * 64 + wave * 8 + (u >> 1) * 4 + (u & 1)) * 2048 + (cid & 1) * 1024;
            __builtin_amdgcn_raw_buffer_store_b128(o_re, rws, lane * 16, o, SC1);
            __builtin_amdgcn_raw_buffer_store_b128(o_im, rws, lane * 16, o + 4096, SC1);
        }
        if (cid == 0) {
            u32x4 lv = {asu(lam.x), TLO, asu(lam.y), TLO};
            __builtin_amdgcn_raw_buffer_store_b128(lv, rws, (p0 + lane) * 32, WS_CH, SC1);
        }
        float sm[2];
#pragma unroll
        for (int pt = 0; pt < 2; ++pt) {
            f32x2 s2 = {0.f, 0.f};
#pragma unroll
            for (int c = 0; c < 4; ++c)
#pragma unroll
                for (int e = 0; e < 4; ++e) { const f32x2 t = {bfl(br[pt][c][e]), bfh(br[pt][c][e])}; s2 += t; }
            sm[pt] = row_sum(s2.x + s2.y);
        }
        if (li == 0) {
            u32x4 sv = {asu(sm[0]), TLO, asu(sm[1]), TLO};
            __builtin_amdgcn_raw_buffer_store_b128(sv, rws, brow * 32 + 16, WS_CH, SC1);
        }
        STAMP(1);
        waitv<0>();
        if (lds_last(L + L_CNT + C_PUB2, 8, lane)) set_flag(rws, WS_FLC + cid * FLS, TLO, THI, lane);
        STAMP(2);
    }

    if (wave < 4) {
        Walk ww(s0 * 16 * (S * 2) + tt * 128, 16 * S * 2, ns, rot);
        if (role == 0) {
            const int kq = xg >> 1, md = j & 3, ft = (xg & 1) * 6 + (j >> 2);
            const rsrc_t rb1 = mk(p.b1, F * 2);
            u32x2 bia[4];
#pragma unroll
            for (int g = 0; g < 4; ++g) bia[g] = __builtin_amdgcn_raw_buffer_load_b64(rb1, (ft * 128 + kq * 32 + 8 * g + 4 * hh) * 2, 0, 0);
            epi_load(ep, p, rws, tt, dt, tid);
            STAMP(11);
            waitv<0>();
            GET_TAG();
            STAMP(1);
            if (wave == 0) poll_flags(rws, WS_FLLN + (md * 128 + kq * 32 + l31) * FLS, TLO, THI);
            lds_add(L + L_CNT + C_GO, lane);
            lds_wait<0>(L + L_CNT + C_GO, 4);
            STAMP(2); CYC(cyc0);
            u32x4 hf[32], wf[4];
            const unsigned ho = WS_H2 + ((2 * md + (wave >> 1)) * 128 + kq * 32) * 2048 + (wave & 1) * 1024;
#pragma unroll
            for (int i = 0; i < 8; ++i) hf[i] = __builtin_amdgcn_raw_buffer_load_b128(rws, lane * 16, ho + i * 2048, SC1);
            LAS char* ta[4];
#pragma unroll
            for (int a = 0; a < 4; ++a) {
                ta[a] = L + (4 * hh + q4) * 256 + (4 * (a ^ kq ^ q4) + 2 * cb + (p4 >> 1)) * 16 + 8 * (p4 & 1);
                acc[a] = (f32x16)(0.f);
            }
            const unsigned t16 = TLO << 16;
            const int px = WS_PX + (md * 11 + ft) * 16 * 9216 + wave * 9216;
            u32x4 gx[9];
            auto send = [&](auto ai) __attribute__((always_inline)) {
                constexpr int a = decltype(ai)::value;
#pragma unroll
                for (int g = 0; g < 3; ++g) {
                    u32x4 o;
                    o.x = pk(acc[a][6 * g], acc[a][6 * g + 1]);
                    o.y = (pk(acc[a][6 * g + 2], 0.f) & 0xffffu) | t16;
                    o.z = g < 2 ? pk(acc[a][g < 2 ? 6 * g + 3 : 15], acc[a][g < 2 ? 6 * g + 4 : 15]) : pk(acc[a][15], 0.f);
                    o.w = (g < 2 ? (pk(acc[a][g < 2 ? 6 * g + 5 : 15], 0.f) & 0xffffu) : 0u) | t16;
                    __builtin_amdgcn_raw_buffer_store_b128(o, rws, lane * 16 + g * 1024, px + (a ^ kq) * 4 * 9216 + (a - 1) * 3072, SC1);
                }
            };
            auto fetch = [&](int k) __attribute__((always_inline)) { gx[k] = __builtin_amdgcn_raw_buffer_load_b128(rws, lane * 16 + (k & 3) * 1024, px + kq * 4 * 9216 + (k >> 2) * 4096, SC1); };
#pragma unroll
            for (int n = 0; n < 4; ++n) wf[n] = trread2(ta[tile_of(n)] + step_of(n) * 4096, ta[tile_of(n)] + step_of(n) * 4096 + 2048);
            SCHED();
            sfor<128>([&](auto ni) __attribute__((always_inline)) {
                constexpr int n = decltype(ni)::value;
                acc[tile_of(n)] = mfma(wf[n & 3], hf[step_of(n)], acc[tile_of(n)]);
                if constexpr (n + 4 < 128) wf[n & 3] = trread2(ta[tile_of(n + 4)] + step_of(n + 4) * 4096, ta[tile_of(n + 4)] + step_of(n + 4) * 4096 + 2048);
                if constexpr (n < 48 && (n & 1)) hf[8 + (n >> 1)] = __builtin_amdgcn_raw_buffer_load_b128(rws, lane * 16, ho + (8 + (n >> 1)) * 2048, SC1);
                if constexpr (n == 47) lds_add(L + L_CNT + C_H2, lane);
                if constexpr (n == 65) send(std::integral_constant<int, 3>{});
                if constexpr (n == 67) send(std::integral_constant<int, 2>{});
                if constexpr (n == 97) send(std::integral_constant<int, 1>{});
                if constexpr (n == 116) { fetch(3); fetch(4); fetch(5); fetch(6); fetch(7); fetch(8); }
                SCHED();
            });
            STAMP(3); CYC(cyc1);
            lds_arrive(L + L_CNT + C_G3, lane);
            fetch(0); fetch(1); fetch(2);
#pragma unroll 1
            for (unsigned sp = 0; sp < SPIN_CAP; ++sp) {
                unsigned bad = 0;
#pragma unroll
                for (int k = 0; k < 9; ++k) bad |= (gx[k].y ^ t16) | (gx[k].w ^ t16);
                if (__builtin_amdgcn_ballot_w64(bad > 0xffffu) == 0) break;
                asm volatile("" ::: "memory");
#pragma unroll
                for (int k = 0; k < 9; ++k) fetch(k);
            }
            STAMP(7);
#pragma unroll
            for (int k = 0; k < 9; ++k) {
                const int g = k % 3;
                acc[0][6 * g] += bfl(gx[k].x); acc[0][6 * g + 1] += bfh(gx[k].x); acc[0][6 * g + 2] += bfl(gx[k].y); acc[0][6 * g + 3] += bfl(gx[k].z);
                if (g < 2) { acc[0][6 * g + 4] += bfh(gx[k].z); acc[0][6 * g + 5] += bfl(gx[k].w); }
            }
            u32x4 uf[2];
#pragma unroll
            for (int g = 0; g < 4; ++g) {
                const float bs[4] = {bfl(bia[g].x), bfh(bia[g].x), bfl(bia[g].y), bfh(bia[g].y)};
                float o[4];
#pragma unroll
                for (int e = 0; e < 4; ++e) {
                    const float a = acc[0][4 * g + e] + bs[e];
                    const float z = a * fmaf(a * a, 0.044715f, 1.0f) * (-2.0f * 0.7978845608028654f * 1.4426950408889634f);
                    o[e] = a * __builtin_amdgcn_rcpf(1.0f + __builtin_amdgcn_exp2f(z));
                }
                uf[g >> 1][(g & 1) * 2] = pk(o[0], o[1]); uf[g >> 1][(g & 1) * 2 + 1] = pk(o[2], o[3]);
            }
            const int uo = WS_U + ((2 * md + (wave >> 1)) * 88 + ft * 8 + kq * 2) * 2048 + (wave & 1) * 1024;
            __builtin_amdgcn_raw_buffer_store_b128(uf[0], rws, lane * 16, uo, SC1);
            __builtin_amdgcn_raw_buffer_store_b128(uf[1], rws, lane * 16, uo + 2048, SC1);
            waitv<0>();
            if (lds_last(L + L_CNT + C_PUB, 4, lane)) {
                lds_wait<0>(L + L_CNT + C_SEEN, 8);
                set_flag(rws, WS_FLU + (md * 44 + ft * 4 + kq) * FLS, TLO, THI, lane);
            }
            STAMP(10);
            lds_wait<0>(L + L_CNT + C_RING, 4);
            ww.cur = ww.lo + (rot + RSL >= ns ? rot + RSL - ns : rot + RSL) * ww.step;
        } else {
            if (role == 1) {
                const rsrc_t rx = mk((const char*)p.x + ct * 128 * 1024, 128 * 1024);
                const rsrc_t rbr = mk((const char*)p.bre + (pg * 128 + wave * 32) * 1024, 32 * 1024);
                const rsrc_t rbi = mk((const char*)p.bim + (pg * 128 + wave * 32) * 1024, 32 * 1024);
                const int tok0 = 64 * hh + 32 * ((lane >> 3) & 1) + 8 * wave + cb;
                const unsigned c0 = ((lane & 7) ^ (lane >> 4)) * 16;
                const unsigned va0 = tok0 * 1024 + c0, va1 = va0 ^ 64, vb0 = (lane >> 3) * 1024 + c0, vb1 = vb0 ^ 64;
                int kt = (ct + 2 * pg) & 7;
                unsigned base = Lb;
#pragma unroll 1
                for (int n = 0; n < 8; ++n) {
                    const unsigned ko = kt * 128;
#pragma unroll
                    for (int i = 0; i < 4; ++i) glds<true>(base + (4 * wave + i) * 1024, (i & 1) ? va1 : va0, rx, ko + i * 2048);
#pragma unroll
                    for (int i = 0; i < 4; ++i) glds<false>(base + 16384 + (8 * wave + i) * 1024, (i & 1) ? vb1 : vb0, rbr, ko + i * 8192);
                    if (n > 0) { waitv<8>(); bar(); }
#pragma unroll
                    for (int i = 0; i < 4; ++i) glds<false>(base + 16384 + (8 * wave + 4 + i) * 1024, (i & 1) ? vb1 : vb0, rbi, ko + i * 8192);
                    kt = (kt + 1) & 7; base = base == Lb + 98304 ? Lb : base + 49152;
                }
                waitv<0>(); bar();
                bar();
                STAMP(1);
                GET_TAG();
            }
            epi_load(ep, p, rws, tt, dt, tid);
#pragma unroll 1
            for (int i = 0; i < RSL; ++i) { w_issue(ringaddr + i * 2048, w2v, rw2, ww); waitv<THR>(); }
            waitv<0>();
        }
        LAS char* ring = L + L_RING + wave * (RSL * 2048);
        const unsigned tr0 = (4 * hh + q4) * 128 + (4 * (q4 >> 1) + 2 * cb + (p4 >> 1)) * 16 + 8 * (p4 & 1), tr1 = tr0 ^ 64;
        u32x4 wp[RSL][2];
#pragma unroll
        for (int i = 0; i < RSL; ++i) {
            wp[i][0] = trread2(ring + i * 2048 + tr0, ring + i * 2048 + tr0 + 1024);
            wp[i][1] = trread2(ring + i * 2048 + tr1, ring + i * 2048 + tr1 + 1024);
        }
        waitl();
#pragma unroll 1
        for (int i = 0; i < RSL; ++i) w_issue(ringaddr + i * 2048, w2v, rw2, ww);
        STAMP(4);
        {
            const int n0 = s0 >> 1, nf = ((s0 + ns - 1) >> 1) - n0 + 1;
            poll_flags(rws, WS_FLU + ((dt >> 1) * 44 + n0 + (lane < nf ? lane : 0)) * FLS, TLO, THI);
        }
        waitv<0>();
        STAMP(5);
        constexpr int DA = 6;
        u32x4 af[DA][2], wf[2][2];
        Walk aw(WS_U + (dt * 88 + s0) * 2048, 2048, ns, rot);
#pragma unroll
        for (int i = 0; i < DA; ++i) {
            const unsigned o = aw.next();
            af[i][0] = __builtin_amdgcn_raw_buffer_load_b128(rws, lane * 16, o, SC1);
            af[i][1] = __builtin_amdgcn_raw_buffer_load_b128(rws, lane * 16 + 1024, o, SC1);
        }
#pragma unroll
        for (int a = 0; a < 4; ++a) acc[a] = (f32x16)(0.f);
        SCHED();
#pragma unroll
        for (int i = 0; i < 22; ++i) {
            if (i + 1 < 22 && i + 1 >= RSL) {
                LAS char* sl = ring + (i + 1 - RSL) * 2048;
                wf[(i + 1) & 1][0] = trread2(sl + tr0, sl + tr0 + 1024);
                wf[(i + 1) & 1][1] = trread2(sl + tr1, sl + tr1 + 1024);
            }
            SCHED();
            if (i + 1 < 22 || wave < 2) {
#pragma unroll
                for (int a = 0; a < 4; ++a) acc[a] = mfma(af[i % DA][a >> 1], i < RSL ? wp[i < RSL ? i : 0][a & 1] : wf[i & 1][a & 1], acc[a]);
            }
            SCHED();
            if (i + DA < 22) {
                const unsigned o = aw.next();
                af[i % DA][0] = __builtin_amdgcn_raw_buffer_load_b128(rws, lane * 16, o, SC1);
                af[i % DA][1] = __builtin_amdgcn_raw_buffer_load_b128(rws, lane * 16 + 1024, o, SC1);
            }
            SCHED();
        }
        STAMP(6);
        if (b == 161 && wave == 0) {
#pragma unroll 1
            for (int i = 0; i < 4; ++i) poll_flags(rws, WS_FLU + (64 * i + lane) * FLS, TLO, THI);
            set_flag(rws, WS_EPOCH, TLO, THI, lane);
        }
    } else {
        __builtin_amdgcn_s_setprio(2);
        const int v = wave - 4, g = lane >> 4, li = lane & 15;
        LAS char* Q = L + L_AUX + v * 16384;
        if (j < 16) {
            const int lg = xg * 16 + j;
            f32x2 f[16];
#pragma unroll
            for (int c = 0; c < 4; ++c)
#pragma unroll
                for (int e = 0; e < 4; ++e) { f[4 * c + e].x = bfl(xr[c][e]); f[4 * c + e].y = bfh(xr[c][e]); }
            STAMP(7);
            f32x2 s2 = f[0];
#pragma unroll
            for (int e = 1; e < 16; ++e) s2 += f[e];
            const float m = row_sum(s2.x + s2.y) * (1.0f / D);
            f32x2 q2 = {0.f, 0.f};
#pragma unroll
            for (int e = 0; e < 16; ++e) { f[e] -= m; q2 += f[e] * f[e]; }
            const float var = row_sum(q2.x + q2.y) * (1.0f / D);
            const float ra = __builtin_amdgcn_rsqf(var + EPS);
            const float sc = ra * __builtin_amdgcn_rsqf(var * ra * ra + EPS);
#pragma unroll
            for (int c = 0; c < 4; ++c) {
                u32x4 o;
#pragma unroll
                for (int e = 0; e < 4; ++e) { const f32x2 t = f[4 * c + e] * sc; o[e] = pk(t.x, t.y); }
                *(LAS u32x4*)(L + L_LN + (4 * v + g) * 1024 + (((li + 16 * c) ^ (g << 2)) * 16)) = o;
            }
            lds_arrive(L + L_CNT + C_LN, lane);
            GET_TAG();
            if (li == 0) {
                u32x4 sv = {asu(m), TLO, asu(ra), TLO};
                __builtin_amdgcn_raw_buffer_store_b128(sv, rws, g * 16, WS_STATS + (lg * 16 + 4 * v) * 16, SC1);
            }
            STAMP(10);
            lds_wait<0>(L + L_CNT + C_LN, 4);
            STAMP(4);
#pragma unroll
            for (int i = 0; i < 4; ++i) {
                LAS char* a = L + L_LN + (4 * hh + q4) * 1024 + (16 * v + 4 * (i ^ q4) + 2 * cb + (p4 >> 1)) * 16 + 8 * (p4 & 1);
                const u32x4 fr = trread2(a, a + 8192);
                __builtin_amdgcn_raw_buffer_store_b128(fr, rws, lane * 16, WS_H2 + ((2 * v + (i >> 1)) * 128 + lg) * 2048 + (i & 1) * 1024, SC1);
            }
            STAMP(1);
            waitv<0>();
            set_flag(rws, WS_FLLN + (v * 128 + lg) * FLS, TLO, THI, lane);
            STAMP(2);
        } else if (j < 24) {
            if (role == 0) { GET_TAG(); }
        } else {
            GET_TAG();
            const int ch = pg * 128 + v * 32 + l31;
            unsigned rd[4];
#pragma unroll
            for (int ks = 0; ks < 4; ++ks) rd[ks] = l31 * 128 + (((2 * ks + hh) ^ ((lane >> 1) & 7)) * 16);
            f32x16 ac[4][2];
#pragma unroll
            for (int a = 0; a < 8; ++a) { ac[a >> 1][a & 1] = (f32x16)(0.f); asm volatile("" : "+v"(ac[a >> 1][a & 1])); }
            u32x4 fr[2][6];
            const unsigned bo = 16384 + v * 8192;
            u32x4 sg0, sg1, cg0, cg1;
            bar();
            STAMP(1); CYC(cyc0);
#pragma unroll
            for (int k = 0; k < 6; ++k) fr[0][k] = *(LAS u32x4*)(L + rd[0] + (k < 2 ? bo + k * 4096 : (k - 2) * 4096));
            unsigned buf = 0;
#pragma unroll 1
            for (int n = 0; n < 8; ++n) {
#pragma unroll
                for (int ks = 0; ks < 4; ++ks) {
                    if (ks == 3) { bar(); buf = buf == 98304 ? 0 : buf + 49152; }
                    LAS char* A = L + buf + rd[(ks + 1) & 3];
                    LAS char* B = A + bo;
                    SCHED();
#pragma unroll
                    for (int k = 0; k < 8; ++k) {
                        ac[k >> 1][k & 1] = mfma(fr[ks & 1][2 + (k >> 1)], fr[ks & 1][k & 1], ac[k >> 1][k & 1]);
                        if (k < 6) fr[(ks + 1) & 1][k] = *(LAS u32x4*)(k < 2 ? B + k * 4096 : A + (k - 2) * 4096);
                        SCHED();
                    }
                }
                if (n == 6) {
                    sg0 = __builtin_amdgcn_raw_buffer_load_b128(rws, lane * 16, WS_STATS + ct * 2048, SC1);
                    sg1 = __builtin_amdgcn_raw_buffer_load_b128(rws, lane * 16 + 1024, WS_STATS + ct * 2048, SC1);
                    cg0 = __builtin_amdgcn_raw_buffer_load_b128(rws, ch * 32, WS_CH, SC1);
                    cg1 = __builtin_amdgcn_raw_buffer_load_b128(rws, ch * 32 + 16, WS_CH, SC1);
                }
            }
            STAMP(2); CYC(cyc1);
#pragma unroll 1
            for (unsigned sp = 0; sp < SPIN_CAP; ++sp) {
                const unsigned bad = (sg0.y ^ tlo) | (sg0.w ^ tlo) | (sg1.y ^ tlo) | (sg1.w ^ tlo) | (cg0.y ^ tlo) | (cg0.w ^ tlo) | (cg1.y ^ tlo) | (cg1.w ^ tlo);
                if (__builtin_amdgcn_ballot_w64(bad != 0) == 0) break;
                __builtin_amdgcn_s_sleep(1);
                asm volatile("" ::: "memory");
                sg0 = __builtin_amdgcn_raw_buffer_load_b128(rws, lane * 16, WS_STATS + ct * 2048, SC1);
                sg1 = __builtin_amdgcn_raw_buffer_load_b128(rws, lane * 16 + 1024, WS_STATS + ct * 2048, SC1);
                cg0 = __builtin_amdgcn_raw_buffer_load_b128(rws, ch * 32, WS_CH, SC1);
                cg1 = __builtin_amdgcn_raw_buffer_load_b128(rws, ch * 32 + 16, WS_CH, SC1);
            }
            {
                LAS float* T = (LAS float*)(Q + l31 * 16 + hh * 4);
                T[0] = asf(sg0.z); T[2] = -asf(sg0.x) * asf(sg0.z);
                T[128] = asf(sg1.z); T[130] = -asf(sg1.x) * asf(sg1.z);
            }
            waitl();
            const f32x2 lam = {asf(cg0.x), asf(cg0.z)};
            const float sbr = asf(cg1.x), sbi = asf(cg1.z);
            f32x2 er = {0.f, 0.f}, ei = {0.f, 0.f};
            f32x4 ab[2][8];
#pragma unroll
            for (int i = 0; i < 8; ++i) ab[0][i] = *(LAS f32x4*)(Q + hh * 512 + i * 16);
#pragma unroll
            for (int m = 0; m < 4; ++m) {
                if (m < 3) {
#pragma unroll
                    for (int i = 0; i < 8; ++i) ab[(m + 1) & 1][i] = *(LAS f32x4*)(Q + hh * 512 + (8 * m + 8 + i) * 16);
                }
                SCHED();
#pragma unroll
                for (int i = 0; i < 8; ++i) {
                    const f32x2 a2 = {ab[m & 1][i].x, ab[m & 1][i].y}, b2 = {ab[m & 1][i].z, ab[m & 1][i].w};
                    const int r = 2 * i;
                    f32x2 vr = {ac[m][0][r], ac[m][0][r + 1]}, vi = {ac[m][1][r], ac[m][1][r + 1]};
                    vr = a2 * vr + b2 * sbr; vi = a2 * vi + b2 * sbi;
                    ac[m][0][r] = vr.x; ac[m][0][r + 1] = vr.y; ac[m][1][r] = vi.x; ac[m][1][r + 1] = vi.y;
                    const f32x2 nr = lam.x * er + (vr - lam.y * ei), ni = lam.x * ei + (vi + lam.y * er);
                    er = nr; ei = ni;
                }
                SCHED();
            }
            f32x2 l32 = lam;
#pragma unroll
            for (int i = 0; i < 5; ++i) l32 = cmul(l32, l32);
            const f32x2 l64 = cmul(l32, l32), l128 = cmul(l64, l64), l256 = cmul(l128, l128);
            const f32x2 e0 = {er.x, ei.x}, e1 = {er.y, ei.y};
            const f32x2 eh = cfma(l32, e0, e1);
            const f32x2 eo = {other_half(eh.x), other_half(eh.y)};
            if (hh == 0) {
                const f32x2 E = cfma(l64, eh, eo);
                u32x4 ge = {asu(E.x), tlo, asu(E.y), tlo};
                __builtin_amdgcn_raw_buffer_store_b128(ge, rws, ch * 16, WS_E + ct * (P * 16), SC1);
            }
            STAMP(3);
            f32x2 H = {0.f, 0.f};
            {
                u32x4 ge[8];
#pragma unroll 1
                for (unsigned sp = 0; sp < SPIN_CAP; ++sp) {
                    asm volatile("" ::: "memory");
                    bool bad = false;
#pragma unroll
                    for (int qq = 0; qq < 8; ++qq) {
                        const int jc = ct - 1 - hh - 2 * qq;
                        ge[qq] = __builtin_amdgcn_raw_buffer_load_b128(rws, jc < 0 ? 0x40000000 : jc * (P * 16) + ch * 16, WS_E, SC1);
                        bad |= jc >= 0 && (ge[qq].y != tlo || ge[qq].w != tlo);
                    }
                    if (__builtin_amdgcn_ballot_w64(bad) == 0) break;
                }
#pragma unroll
                for (int qq = 7; qq >= 0; --qq) { const f32x2 ev = {asf(ge[qq].x), asf(ge[qq].z)}; H = cfma(l256, H, ev); }
                if (hh) H = cmul(H, l128);
                H.x += other_half(H.x); H.y += other_half(H.y);
            }
            STAMP(4);
            if (hh) H = cfma(l64, H, eo);
            const f32x2 H1 = cfma(l32, H, e0);
            f32x2 hr = {H.x, H1.x}, hi = {H.y, H1.y};
            const int fw = ((l31 & 3) << 2) | ((l31 >> 1) & 3);
#pragma unroll
            for (int m = 0; m < 4; ++m) {
                u32x4 o[4];
#pragma unroll
                for (int i2 = 0; i2 < 4; ++i2) {
                    const int r = 4 * i2;
                    const f32x2 vr0 = {ac[m][0][r], ac[m][0][r + 1]}, vi0 = {ac[m][1][r], ac[m][1][r + 1]};
                    const f32x2 vr1 = {ac[m][0][r + 2], ac[m][0][r + 3]}, vi1 = {ac[m][1][r + 2], ac[m][1][r + 3]};
                    const f32x2 ar = lam.x * hr + (vr0 - lam.y * hi), ai = lam.x * hi + (vi0 + lam.y * hr);
                    hr = lam.x * ar + (vr1 - lam.y * ai); hi = lam.x * ai + (vi1 + lam.y * ar);
                    o[0][i2] = pk(ar.x, hr.x); o[1][i2] = pk(ar.y, hr.y); o[2][i2] = pk(ai.x, hi.x); o[3][i2] = pk(ai.y, hi.y);
                }
#pragma unroll
                for (int u = 0; u < 4; ++u)
                    *(LAS u32x4*)(Q + (u >> 1) * 8192 + l31 * 256 + (((8 * hh + 4 * (u & 1) + m) ^ fw) * 16)) = o[u];
            }
            waitl();
            {
                const int r0 = 8 * hh + q4, f0 = (q4 << 2) | ((r0 >> 1) & 3), f1 = (q4 << 2) | (((r0 + 4) >> 1) & 3);
                const int ho = WS_H + (2 * ct * 64 + pg * 16 + v * 4) * 2048;
                u32x4 fo[16];
#pragma unroll
                for (int tq = 0; tq < 4; ++tq) {
                    const int c = 4 * tq + 2 * cb + (p4 >> 1);
                    LAS char* a0 = Q + r0 * 256 + ((c ^ f0) * 16) + 8 * (p4 & 1);
                    LAS char* a1 = Q + (r0 + 4) * 256 + ((c ^ f1) * 16) + 8 * (p4 & 1);
#pragma unroll
                    for (int kk = 0; kk < 4; ++kk) fo[tq * 4 + kk] = trread2(a0 + kk * 4096, a1 + kk * 4096);
                }
                SCHED();
#pragma unroll
                for (int k = 0; k < 16; ++k)
                    __builtin_amdgcn_raw_buffer_store_b128(fo[k], rws, lane * 16 + ((k >> 2) & 1) * 1024, ho + ((k >> 3) * 64 + (k & 3)) * 2048, SC1);
            }
            STAMP(7);
            waitv<0>();
            if (lds_last(L + L_CNT + C_PUB, 4, lane)) {
                lds_wait<0>(L + L_CNT + C_SEEN, 8);
                set_flag(rws, WS_FLH + (ct * 4 + pg) * FLS, tlo, THI, lane);
            }
            STAMP(10);
        }
        __builtin_amdgcn_s_setprio(0);
        constexpr int CP = 14, HD = 5;
        if (role == 0) lds_wait<2>(L + L_CNT + C_SEEN, 8);
        epi_load(ep, p, rws, tt, dt, tid);
        if (role == 0) lds_wait<2>(L + L_CNT + C_H2, 4);
        poll_flags(rws, WS_FLC + (2 * dt + (lane & 1)) * FLS, TLO, THI);
        u32x4 cf[CP][2], hf[HD][2];
        const unsigned co = WS_CT + (dt * 64 + v * 16) * 2048, ho = WS_H + (tt * 64 + v * 16) * 2048;
#pragma unroll
        for (int i = 0; i < CP; ++i) {
            cf[i][0] = __builtin_amdgcn_raw_buffer_load_b128(rws, lane * 16, co + i * 2048, SC1);
            cf[i][1] = __builtin_amdgcn_raw_buffer_load_b128(rws, lane * 16 + 1024, co + i * 2048, SC1);
            waitv<THR>();
        }
        SCHED();
        STAMP(11);
        if (role == 0) {
            Walk ww(s0 * 16 * (S * 2) + tt * 128, 16 * S * 2, ns, rot);
            lds_wait<2>(L + L_CNT + C_G3, 4);
#pragma unroll 1
            for (int i = 0; i < RSL; ++i) { w_issue(ringaddr + i * 2048, w2v, rw2, ww); waitv<THR>(); }
            waitv<0>();
            lds_arrive(L + L_CNT + C_RING, lane);
            STAMP(3);
        }
        poll_flags(rws, WS_FLH + ((tt >> 1) * 4 + v) * FLS, TLO, THI);
        STAMP(5);
#pragma unroll
        for (int i = 0; i < HD; ++i) {
            hf[i][0] = __builtin_amdgcn_raw_buffer_load_b128(rws, lane * 16, ho + i * 2048, SC1);
            hf[i][1] = __builtin_amdgcn_raw_buffer_load_b128(rws, lane * 16 + 1024, ho + i * 2048, SC1);
        }
#pragma unroll
        for (int a = 0; a < 4; ++a) acc[a] = (f32x16)(0.f);
        SCHED();
#pragma unroll
        for (int i = 0; i < 16; ++i) {
#pragma unroll
            for (int a = 0; a < 4; ++a) acc[a] = mfma(cf[i % CP][a >> 1], hf[i % HD][a & 1], acc[a]);
            SCHED();
            if (i + CP < 16) {
                cf[i % CP][0] = __builtin_amdgcn_raw_buffer_load_b128(rws, lane * 16, co + (i + CP) * 2048, SC1);
                cf[i % CP][1] = __builtin_amdgcn_raw_buffer_load_b128(rws, lane * 16 + 1024, co + (i + CP) * 2048, SC1);
            }
            if (i + HD < 16) {
                hf[i % HD][0] = __builtin_amdgcn_raw_buffer_load_b128(rws, lane * 16, ho + (i + HD) * 2048, SC1);
                hf[i % HD][1] = __builtin_amdgcn_raw_buffer_load_b128(rws, lane * 16 + 1024, ho + (i + HD) * 2048, SC1);
            }
            SCHED();
        }
        STAMP(6);
        if (role == 0) lds_wait<0>(L + L_CNT + C_RING, 4);
    }
    {
        LAS char* I = L + (wave < 4 ? L_RING + wave * (RSL * 2048) : L_AUX + (wave - 4) * 16384);
        const int sw = (l31 & 7) ^ (((l31 >> 1) & 1) << 3);
#pragma unroll
        for (int a = 0; a < 4; ++a)
#pragma unroll
            for (int g = 0; g < 4; ++g) {
                f32x4 v = {acc[a][4 * g], acc[a][4 * g + 1], acc[a][4 * g + 2], acc[a][4 * g + 3]};
                *(LAS f32x4*)(I + ((a & 1) * 32 + l31) * 256 + ((((a >> 1) * 8 + 2 * g + hh) ^ sw) * 16)) = v;
            }
    }
    bar();
    STAMP(8);
    {
        const int t = tid >> 3, sw = (t & 7) ^ (((t >> 1) & 1) << 3);
        const int et = tt * 64 + t, ed = dt * 64 + (tid & 7) * 4;
        if (ep.sg.y != tlo || ep.sg.w != tlo) ep.sg = __builtin_amdgcn_raw_buffer_load_b128(rws, et * 16, WS_STATS, SC1);
        const float mean = asf(ep.sg.x), rstd = asf(ep.sg.z);
#pragma unroll
        for (int h2 = 0; h2 < 2; ++h2) {
            LAS char* R = L + t * 256 + ((((tid & 7) + 8 * h2) ^ sw) * 16);
            f32x4 y2 = *(LAS f32x4*)(R + L_RING), y = *(LAS f32x4*)(R + L_AUX);
#pragma unroll
            for (int w = 1; w < 4; ++w) { y2 += *(LAS f32x4*)(R + L_RING + w * (RSL * 2048)); y += *(LAS f32x4*)(R + L_AUX + w * 16384); }
            const u32x2 xx = h2 ? ep.x1 : ep.x0, kk = h2 ? ep.k1 : ep.k0;
            const float xf[4] = {bfl(xx.x), bfh(xx.x), bfl(xx.y), bfh(xx.y)};
            const float kf[4] = {bfl(kk.x), bfh(kk.x), bfl(kk.y), bfh(kk.y)};
            f32x4 o;
#pragma unroll
            for (int e = 0; e < 4; ++e) {
                const float z = y2[e] + ep.b2;
                const float g = z * __builtin_amdgcn_rcpf(1.0f + __builtin_amdgcn_exp2f(z * -1.4426950408889634f));
                const float s = fmaf((xf[e] - mean) * rstd, kf[e], y[e]);
                o[e] = fmaf(s, g, xf[e]);
            }
            *(GAS f32x4*)((GAS char*)p.out + (et * D + ed + h2 * 32) * 4) = o;
        }
    }
    STAMP(9);
}

extern "C" void kernel_launch(void* const* d_in, const int* in_sizes, int n_in, void* d_out, int out_size, void* d_ws, size_t ws_size,
                              hipStream_t stream) {
    Params p;
    p.x = d_in[0]; p.lre = d_in[1]; p.lim = d_in[2]; p.bre = d_in[3]; p.bim = d_in[4]; p.cre = d_in[5]; p.cim = d_in[6];
    p.ldt = d_in[7]; p.dsk = d_in[8]; p.w1 = d_in[10]; p.b1 = d_in[11]; p.w2 = d_in[12]; p.b2 = d_in[13];
    p.out = d_out; p.ws = d_ws;
    fused_kernel<<<256, 512, 0, stream>>>(p);
}
```

```cpp
#define PROBE 7
#define PROLE 0
#define PGRP 1
#define PJ 16
#include <hip/hip_runtime.h>
#include <hip/hip_bf16.h>
#include <stdint.h>
#include <utility>

#define LAS __attribute__((address_space(3)))
#define GAS __attribute__((address_space(1)))
#define CAS __attribute__((address_space(4)))
#define DI __device__ __forceinline__
typedef short s16x4 __attribute__((ext_vector_type(4)));
typedef short s16x8 __attribute__((ext_vector_type(8)));
typedef __bf16 bf16x8 __attribute__((ext_vector_type(8)));
typedef __bf16 bf16x2 __attribute__((ext_vector_type(2)));
typedef float f32x2 __attribute__((ext_vector_type(2)));
typedef float f32x4 __attribute__((ext_vector_type(4)));
typedef float f32x16 __attribute__((ext_vector_type(16)));
typedef unsigned u32x2 __attribute__((ext_vector_type(2)));
typedef unsigned u32x4 __attribute__((ext_vector_type(4)));
typedef __amdgpu_buffer_rsrc_t rsrc_t;

constexpr int S = 2048, D = 512, P = 512, F = 1376;
constexpr float EPS = 1e-5f;
constexpr int SC1 = 16;
constexpr int FLS = 128;
constexpr int WS_EPOCH = 0;
constexpr int WS_FLU = 4096;
constexpr int WS_FLH = WS_FLU + 176 * FLS;
constexpr int WS_FLC = WS_FLH + 64 * FLS;
constexpr int WS_FLLN = WS_FLC + 16 * FLS;
constexpr int WS_STATS = 256 * 1024;
constexpr int WS_CH = WS_STATS + S * 16;
constexpr int WS_E = WS_CH + P * 32;
constexpr int WS_H2 = 1 << 20;
constexpr int WS_U = WS_H2 + 8 * 128 * 2048;
constexpr int WS_CT = WS_U + 8 * 88 * 2048;
constexpr int WS_H = WS_CT + 8 * 64 * 2048;
constexpr int WS_PX = WS_H + 32 * 64 * 2048;
constexpr int WS_END = WS_PX + 44 * 16 * 9216;
constexpr int WS_STAMP = 32 << 20;
static_assert(WS_FLLN + 512 * FLS <= WS_STATS && WS_E + 16 * P * 16 <= WS_H2 && WS_END <= WS_STAMP, "ws");
constexpr int RSL = 11;
constexpr int L_RING = 0;
constexpr int L_AUX = 4 * RSL * 2048;
constexpr int L_LN = 131072;
constexpr int L_DUMMY = L_AUX + 65536;
constexpr int L_CNT = L_DUMMY + 4096;
constexpr int LDS_BYTES = 163840;
static_assert(L_CNT + 64 <= LDS_BYTES && 3 * 49152 <= L_DUMMY && L_LN + 16384 <= L_DUMMY, "lds");
constexpr int C_SEEN = 0, C_LN = 4, C_LNST = 8, C_G3 = 12, C_RING = 16, C_PUB = 20, C_PUB2 = 24, C_GO = 28, C_XL = 32, C_H1 = 36, C_H2 = 40;
constexpr unsigned SPIN_CAP = 1u << 17;

struct Params {
    const void* x; const void* lre; const void* lim; const void* bre; const void* bim; const void* cre; const void* cim;
    const void* ldt; const void* dsk; const void* w1; const void* b1; const void* w2; const void* b2;
    void* out; void* ws;
};

DI rsrc_t mk(const void* p, int bytes) { return __builtin_amdgcn_make_buffer_rsrc((void*)p, 0, bytes, 0x00020000); }
DI float asf(unsigned v) { return __builtin_bit_cast(float, v); }
DI unsigned asu(float v) { return __builtin_bit_cast(unsigned, v); }
DI bf16x2 asb2(unsigned v) { return __builtin_bit_cast(bf16x2, v); }
DI float bfl(unsigned v) { return __builtin_bit_cast(float, v << 16); }
DI float bfh(unsigned v) { return __builtin_bit_cast(float, v & 0xffff0000u); }
DI unsigned pk(float lo, float hi) { f32x2 v = {lo, hi}; return __builtin_bit_cast(unsigned, __builtin_convertvector(v, bf16x2)); }
DI float ldbf(const void* p, int i) { return bfl(((const GAS unsigned short*)p)[i]); }
DI f32x16 mfma(u32x4 a, u32x4 b, f32x16 c) {
    return __builtin_amdgcn_mfma_f32_32x32x16_bf16(__builtin_bit_cast(bf16x8, a), __builtin_bit_cast(bf16x8, b), c, 0, 0, 0);
}
DI u32x4 trread2(LAS char* lo, LAS char* hi) {
    s16x4 a = __builtin_amdgcn_ds_read_tr16_b64_v4i16((LAS s16x4*)lo);
    s16x4 b = __builtin_amdgcn_ds_read_tr16_b64_v4i16((LAS s16x4*)hi);
    s16x8 r = __builtin_shufflevector(a, b, 0, 1, 2, 3, 4, 5, 6, 7);
    return __builtin_bit_cast(u32x4, r);
}
template <bool NTL> DI void glds(unsigned ldsaddr, unsigned voff, rsrc_t rs, unsigned soff) {
    if (NTL) asm volatile("s_mov_b32 m0, %0\n\ts_nop 0\n\tbuffer_load_dwordx4 %1, %2, %3 offen nt lds" ::"s"(ldsaddr), "v"(voff), "s"(rs), "s"(soff) : "memory");
    else     asm volatile("s_mov_b32 m0, %0\n\ts_nop 0\n\tbuffer_load_dwordx4 %1, %2, %3 offen lds" ::"s"(ldsaddr), "v"(voff), "s"(rs), "s"(soff) : "memory");
}
template <int N> DI void waitv() { asm volatile("s_waitcnt vmcnt(%0)" ::"i"(N) : "memory"); }
DI void waitl() { asm volatile("s_waitcnt lgkmcnt(0)" ::: "memory"); }
DI void bar() { asm volatile("s_waitcnt lgkmcnt(0)\n\ts_barrier" ::: "memory"); }
#define SCHED() __builtin_amdgcn_sched_barrier(0)
template <int CTRL> DI float dpp(float v) { return asf((unsigned)__builtin_amdgcn_update_dpp(0, (int)asu(v), CTRL, 0xf, 0xf, true)); }
DI float row_sum(float v) {
    v += dpp<0x128>(v); v += dpp<0x124>(v); v += dpp<0x122>(v); v += dpp<0x121>(v);
    return v;
}
DI float other_half(float v) { return __shfl_xor(v, 32); }
DI void poll_flags(rsrc_t ws, int o, unsigned tlo, unsigned thi) {
#pragma unroll 1
    for (unsigned sp = 0; sp < SPIN_CAP; ++sp) {
        asm volatile("" ::: "memory");
        u32x2 v = __builtin_amdgcn_raw_buffer_load_b64(ws, o, 0, SC1);
        if (__builtin_amdgcn_ballot_w64(v.x != tlo || v.y != thi) == 0) break;
        __builtin_amdgcn_s_sleep(1);
    }
    asm volatile("" ::: "memory");
}
DI void lds_add(LAS char* cnt, int lane) {
    asm volatile("ds_add_u32 %0, %1" ::"v"((unsigned)(size_t)cnt + (lane ? 2044 + lane * 4 : 0)), "v"(1u) : "memory");
}
DI void lds_arrive(LAS char* cnt, int lane) { waitl(); lds_add(cnt, lane); }
template <int SLEEP> DI void lds_wait(LAS char* cnt, unsigned n) {
#pragma unroll 1
    for (unsigned sp = 0; sp < (SPIN_CAP << 4); ++sp) {
        asm volatile("" ::: "memory");
        if ((unsigned)__builtin_amdgcn_readfirstlane(*(volatile LAS unsigned*)cnt) >= n) break;
        if (SLEEP) __builtin_amdgcn_s_sleep(SLEEP);
    }
    asm volatile("" ::: "memory");
}
DI bool lds_last(LAS char* cnt, unsigned n, int lane) {
    unsigned old = 0;
    if (lane == 0) old = __hip_atomic_fetch_add((LAS unsigned*)cnt, 1u, __ATOMIC_RELAXED, __HIP_MEMORY_SCOPE_WORKGROUP);
    asm volatile("" ::: "memory");
    return (unsigned)__builtin_amdgcn_readfirstlane(old) == n - 1;
}
DI void set_flag(rsrc_t ws, int off, unsigned tlo, unsigned thi, int lane) {
    if (lane == 0) { u32x2 v = {tlo, thi}; __builtin_amdgcn_raw_buffer_store_b64(v, ws, off, 0, SC1); }
}
DI f32x2 cmul(f32x2 a, f32x2 b) { f32x2 r; r.x = a.x * b.x - a.y * b.y; r.y = a.x * b.y + a.y * b.x; return r; }
DI f32x2 cfma(f32x2 a, f32x2 b, f32x2 c) {
    f32x2 r; r.x = fmaf(a.x, b.x, fmaf(-a.y, b.y, c.x)); r.y = fmaf(a.x, b.y, fmaf(a.y, b.x, c.y)); return r;
}
DI void lam_of(const Params& p, int ch, f32x2& lam, f32x2& Lam) {
    const float dt0 = expf(ldbf(p.ldt, ch));
    const float dt = bfl(pk(dt0, dt0));
    Lam.x = ldbf(p.lre, ch); Lam.y = ldbf(p.lim, ch);
    const float e = expf(dt * Lam.x);
    float sn, cs; sincosf(dt * Lam.y, &sn, &cs);
    lam.x = e * cs; lam.y = e * sn;
}

#define STAMP(k) do { if ((k) == 0) pr0 = __builtin_amdgcn_s_memrealtime(); if ((k) == PROBE) pr1 = __builtin_amdgcn_s_memrealtime(); } while (0)
#define CYC(v) do { } while (0)

#define W2NT false
#define STHR 5
#define THR 6
#define CODE_LEN 32256
template <class Fn, int... I> DI void sfor_impl(Fn&& f, std::integer_sequence<int, I...>) { (f(std::integral_constant<int, I>{}), ...); }
template <int N, class Fn> DI void sfor(Fn&& f) { sfor_impl(f, std::make_integer_sequence<int, N>{}); }
constexpr int tile_of(int n) { return n < 64 ? 3 - (n & 1) : (n < 96 ? 1 : 0); }
constexpr int step_of(int n) { return n < 64 ? n >> 1 : (n & 31); }
struct Walk {
    unsigned cur, lo, hi, step;
    DI Walk(unsigned base, unsigned st, int n, int rot) : cur(base + rot * st), lo(base), hi(base + n * st), step(st) {}
    DI unsigned next() { const unsigned r = cur; cur += step; cur = cur == hi ? lo : cur; return r; }
};
DI void w_issue(unsigned slotaddr, unsigned wv, rsrc_t rw, Walk& w) {
    const unsigned o = w.next();
    glds<W2NT>(slotaddr, wv, rw, o);
    glds<W2NT>(slotaddr + 1024, wv, rw, o + 8 * S * 2);
}
struct Epi { u32x2 x0, x1, k0, k1; u32x4 sg; float b2; };
DI void epi_load(Epi& e, const Params& p, rsrc_t rws, int tt, int dt, int tid) {
    const int et = tt * 64 + (tid >> 3), ed = dt * 64 + (tid & 7) * 4;
    e.x0 = *(const GAS u32x2*)((const GAS char*)p.x + (et * D + ed) * 2);
    e.x1 = *(const GAS u32x2*)((const GAS char*)p.x + (et * D + ed + 32) * 2);
    e.k0 = *(const GAS u32x2*)((const GAS char*)p.dsk + ed * 2);
    e.k1 = *(const GAS u32x2*)((const GAS char*)p.dsk + (ed + 32) * 2);
    e.b2 = ldbf(p.b2, et);
    e.sg = __builtin_amdgcn_raw_buffer_load_b128(rws, et * 16, WS_STATS, SC1);
}

__global__ void __launch_bounds__(512, 2) fused_kernel(Params p) {
    __shared__ __attribute__((aligned(16))) char smem[LDS_BYTES];
    LAS char* L = (LAS char*)smem;
    const unsigned Lb = (unsigned)(size_t)L;
    const int b = blockIdx.x, xg = b & 7, j = b >> 3;
    const int role = j >= 24 ? 1 : (((xg & 1) && j >= 20) ? 2 : 0);
    const int tid = threadIdx.x ^ (role == 1 ? 0 : 256), lane = tid & 63, wave = __builtin_amdgcn_readfirstlane(tid >> 6);
    unsigned long long pr0 = 0, pr1 = 0;
    STAMP(0);
    {
        unsigned long long pc;
        constexpr unsigned len = CODE_LEN;
        asm volatile("s_getpc_b64 %0" : "=s"(pc));
        const rsrc_t rc = mk((const void*)(pc & ~255ull), len);
        const unsigned piece = ((j - 16) * 8 + wave) * 1024;
        if (piece < len) glds<false>(Lb + L_DUMMY + (wave & 3) * 1024, lane * 16, rc, piece);
    }
    u32x4 xr[4];
    if (j < 16 && wave >= 4) {
#pragma unroll
        for (int c = 0; c < 4; ++c)
            xr[c] = __builtin_nontemporal_load((const GAS u32x4*)((const GAS char*)p.x + (xg * 256 + j * 16 + 4 * (wave - 4) + (lane >> 4)) * 1024 + ((lane & 15) + 16 * c) * 16));
    }
    const rsrc_t rws = mk(p.ws, WS_END);
    const u32x2 eraw = __builtin_amdgcn_raw_buffer_load_b64(rws, WS_EPOCH, 0, SC1);
    if (threadIdx.x < 16) *(LAS unsigned*)(L + L_CNT + threadIdx.x * 4) = 0u;
    bar();
    STAMP(12);
    if (role == 0 && wave < 4) {
        const rsrc_t rw1 = mk(p.w1, S * F * 2);
        const unsigned wv = (lane >> 4) * (F * 2) + ((lane & 15) ^ ((lane >> 4) << 2)) * 16;
        const unsigned wb = ((xg >> 1) * 512 + wave * 4) * (F * 2) + ((xg & 1) * 6 + (j >> 2)) * 256;
#pragma unroll 1
        for (int i = 0; i < 32; ++i) {
            const int ks = (i + 8 * j) & 31;
            glds<true>(Lb + ks * 4096 + wave * 1024, wv, rw1, wb + ks * 16 * (F * 2));
            waitv<STHR>();
        }
    }
    if (j == 16 && wave >= 4) {
        const rsrc_t rv = wave == 4 ? mk(p.b1, F * 2) : (wave == 5 ? mk(p.dsk, D * 2) : mk((const char*)p.b2 + xg * 512, 512));
#pragma unroll 1
        for (int i = 0; i < 3; ++i) glds<false>(Lb + L_DUMMY + (wave & 3) * 1024, lane * 16, rv, i * 1024);
    }
    unsigned tlo, thi;
#define GET_TAG()                                                                     \
    tlo = __builtin_amdgcn_readfirstlane(eraw.x) + 1u;                                \
    thi = __builtin_amdgcn_readfirstlane(eraw.y);                                     \
    asm volatile("" ::"s"(tlo), "s"(thi));                                            \
    lds_add(L + L_CNT + C_SEEN, lane)
#define TLO tlo
#define THI thi
    const int hh = lane >> 5, l31 = lane & 31, cb = (lane >> 4) & 1, q4 = (lane >> 2) & 3, p4 = lane & 3;
    const int tt = xg * 4 + (j >> 3), dt = j & 7;
    const int ct = xg * 2 + ((j >> 2) & 1), pg = j & 3;
    const rsrc_t rw2 = mk(p.w2, F * S * 2);
    const int wq = wave & 3;
    const int s0 = wq < 2 ? 22 * wq : 21 * wq + 2, ns = wq < 2 ? 22 : 21;
    const int rot = (dt * 21) >> 3;
    const unsigned w2v = (lane >> 3) * (S * 2) + ((lane & 7) ^ (cb << 2)) * 16;
    const unsigned ringaddr = Lb + L_RING + wq * (RSL * 2048);
    f32x16 acc[4];
    Epi ep;

    if (role == 2) {
        const int cid = (xg >> 1) * 4 + (j - 20), g = lane >> 4, li = lane & 15, p0 = wave * 64;
        u32x4 cr[4], ci[4], br[2][4];
#pragma unroll
        for (int u = 0; u < 4; ++u) {
            const int o = (cid * 32 + l31) * 1024 + (p0 + u * 16 + 8 * hh) * 2;
            cr[u] = *(const GAS u32x4*)((const GAS char*)p.cre + o);
            ci[u] = *(const GAS u32x4*)((const GAS char*)p.cim + o);
        }
        f32x2 lam, Lam;
        lam_of(p, p0 + lane, lam, Lam);
        GET_TAG();
        const int brow = cid * 32 + wave * 4 + g;
#pragma unroll
        for (int c = 0; c < 8; ++c)
            br[c >> 2][c & 3] = *(const GAS u32x4*)((const GAS char*)(c < 4 ? p.bre : p.bim) + brow * 1024 + (li + 16 * (c & 3)) * 16);
        const float inv = 2.0f / (Lam.x * Lam.x + Lam.y * Lam.y);
        f32x2 cf = {((lam.x - 1.0f) * Lam.x + lam.y * Lam.y) * inv, (lam.y * Lam.x - (lam.x - 1.0f) * Lam.y) * inv};
        LAS char* K = L + (wave < 4 ? L_RING + wave * (RSL * 2048) : L_AUX + (wave - 4) * 16384);
        *(LAS f32x2*)(K + lane * 8) = cf;
        waitl();
#pragma unroll
        for (int u = 0; u < 4; ++u) {
            u32x4 o_re, o_im;
#pragma unroll
            for (int e = 0; e < 4; ++e) {
                const f32x4 k2 = *(LAS f32x4*)(K + (u * 16 + 8 * hh + 2 * e) * 8);
                const float a0 = bfl(cr[u][e]), b0 = bfl(ci[u][e]), a1 = bfh(cr[u][e]), b1 = bfh(ci[u][e]);
                o_re[e] = pk(a0 * k2.x - b0 * k2.y, a1 * k2.z - b1 * k2.w);
                o_im[e] = pk(-(a0 * k2.y + b0 * k2.x), -(a1 * k2.w + b1 * k2.z));
            }
            const int o = WS_CT + ((cid >> 1) * 64 + wave * 8 + (u >> 1) * 4 + (u & 1)) * 2048 + (cid & 1) * 1024;
            __builtin_amdgcn_raw_buffer_store_b128(o_re, rws, lane * 16, o, SC1);
            __builtin_amdgcn_raw_buffer_store_b128(o_im, rws, lane * 16, o + 4096, SC1);
        }
        if (cid == 0) {
            u32x4 lv = {asu(lam.x), TLO, asu(lam.y), TLO};
            __builtin_amdgcn_raw_buffer_store_b128(lv, rws, (p0 + lane) * 32, WS_CH, SC1);
        }
        float sm[2];
#pragma unroll
        for (int pt = 0; pt < 2; ++pt) {
            f32x2 s2 = {0.f, 0.f};
#pragma unroll
            for (int c = 0; c < 4; ++c)
#pragma unroll
                for (int e = 0; e < 4; ++e) { const f32x2 t = {bfl(br[pt][c][e]), bfh(br[pt][c][e])}; s2 += t; }
            sm[pt] = row_sum(s2.x + s2.y);
        }
        if (li == 0) {
            u32x4 sv = {asu(sm[0]), TLO, asu(sm[1]), TLO};
            __builtin_amdgcn_raw_buffer_store_b128(sv, rws, brow * 32 + 16, WS_CH, SC1);
        }
        STAMP(1);
        waitv<0>();
        if (lds_last(L + L_CNT + C_PUB2, 8, lane)) set_flag(rws, WS_FLC + cid * FLS, TLO, THI, lane);
        STAMP(2);
    }

    if (wave < 4) {
        Walk ww(s0 * 16 * (S * 2) + tt * 128, 16 * S * 2, ns, rot);
        if (role == 0) {
            const int kq = xg >> 1, md = j & 3, ft = (xg & 1) * 6 + (j >> 2);
            const rsrc_t rb1 = mk(p.b1, F * 2);
            u32x2 bia[4];
#pragma unroll
            for (int g = 0; g < 4; ++g) bia[g] = __builtin_amdgcn_raw_buffer_load_b64(rb1, (ft * 128 + kq * 32 + 8 * g + 4 * hh) * 2, 0, 0);
            epi_load(ep, p, rws, tt, dt, tid);
            STAMP(11);
            waitv<0>();
            GET_TAG();
            STAMP(1);
            if (wave == 0) poll_flags(rws, WS_FLLN + (md * 128 + kq * 32 + l31) * FLS, TLO, THI);
            lds_add(L + L_CNT + C_GO, lane);
            lds_wait<0>(L + L_CNT + C_GO, 4);
            STAMP(2); CYC(cyc0);
            u32x4 hf[32], wf[4];
            const unsigned ho = WS_H2 + ((2 * md + (wave >> 1)) * 128 + kq * 32) * 2048 + (wave & 1) * 1024;
#pragma unroll
            for (int i = 0; i < 8; ++i) hf[i] = __builtin_amdgcn_raw_buffer_load_b128(rws, lane * 16, ho + i * 2048, SC1);
            LAS char* ta[4];
#pragma unroll
            for (int a = 0; a < 4; ++a) {
                ta[a] = L + (4 * hh + q4) * 256 + (4 * (a ^ kq ^ q4) + 2 * cb + (p4 >> 1)) * 16 + 8 * (p4 & 1);
                acc[a] = (f32x16)(0.f);
            }
            const unsigned t16 = TLO << 16;
            const int px = WS_PX + (md * 11 + ft) * 16 * 9216 + wave * 9216;
            u32x4 gx[9];
            auto send = [&](auto ai) __attribute__((always_inline)) {
                constexpr int a = decltype(ai)::value;
#pragma unroll
                for (int g = 0; g < 3; ++g) {
                    u32x4 o;
                    o.x = pk(acc[a][6 * g], acc[a][6 * g + 1]);
                    o.y = (pk(acc[a][6 * g + 2], 0.f) & 0xffffu) | t16;
                    o.z = g < 2 ? pk(acc[a][g < 2 ? 6 * g + 3 : 15], acc[a][g < 2 ? 6 * g + 4 : 15]) : pk(acc[a][15], 0.f);
                    o.w = (g < 2 ? (pk(acc[a][g < 2 ? 6 * g + 5 : 15], 0.f) & 0xffffu) : 0u) | t16;
                    __builtin_amdgcn_raw_buffer_store_b128(o, rws, lane * 16 + g * 1024, px + (a ^ kq) * 4 * 9216 + (a - 1) * 3072, SC1);
                }
            };
            auto fetch = [&](int k) __attribute__((always_inline)) { gx[k] = __builtin_amdgcn_raw_buffer_load_b128(rws, lane * 16 + (k & 3) * 1024, px + kq * 4 * 9216 + (k >> 2) * 4096, SC1); };
#pragma unroll
            for (int n = 0; n < 4; ++n) wf[n] = trread2(ta[tile_of(n)] + step_of(n) * 4096, ta[tile_of(n)] + step_of(n) * 4096 + 2048);
            SCHED();
            sfor<128>([&](auto ni) __attribute__((always_inline)) {
                constexpr int n = decltype(ni)::value;
                acc[tile_of(n)] = mfma(wf[n & 3], hf[step_of(n)], acc[tile_of(n)]);
                if constexpr (n + 4 < 128) wf[n & 3] = trread2(ta[tile_of(n + 4)] + step_of(n + 4) * 4096, ta[tile_of(n + 4)] + step_of(n + 4) * 4096 + 2048);
                if constexpr (n < 48 && (n & 1)) hf[8 + (n >> 1)] = __builtin_amdgcn_raw_buffer_load_b128(rws, lane * 16, ho + (8 + (n >> 1)) * 2048, SC1);
                if constexpr (n == 47) lds_add(L + L_CNT + C_H2, lane);
                if constexpr (n == 65) send(std::integral_constant<int, 3>{});
                if constexpr (n == 67) send(std::integral_constant<int, 2>{});
                if constexpr (n == 97) send(std::integral_constant<int, 1>{});
                if constexpr (n == 116) { fetch(3); fetch(4); fetch(5); fetch(6); fetch(7); fetch(8); }
                SCHED();
            });
            STAMP(3); CYC(cyc1);
            lds_arrive(L + L_CNT + C_G3, lane);
            fetch(0); fetch(1); fetch(2);
#pragma unroll 1
            for (unsigned sp = 0; sp < SPIN_CAP; ++sp) {
                unsigned bad = 0;
#pragma unroll
                for (int k = 0; k < 9; ++k) bad |= (gx[k].y ^ t16) | (gx[k].w ^ t16);
                if (__builtin_amdgcn_ballot_w64(bad > 0xffffu) == 0) break;
                asm volatile("" ::: "memory");
#pragma unroll
                for (int k = 0; k < 9; ++k) fetch(k);
            }
            STAMP(7);
#pragma unroll
            for (int k = 0; k < 9; ++k) {
                const int g = k % 3;
                acc[0][6 * g] += bfl(gx[k].x); acc[0][6 * g + 1] += bfh(gx[k].x); acc[0][6 * g + 2] += bfl(gx[k].y); acc[0][6 * g + 3] += bfl(gx[k].z);
                if (g < 2) { acc[0][6 * g + 4] += bfh(gx[k].z); acc[0][6 * g + 5] += bfl(gx[k].w); }
            }
            u32x4 uf[2];
#pragma unroll
            for (int g = 0; g < 4; ++g) {
                const float bs[4] = {bfl(bia[g].x), bfh(bia[g].x), bfl(bia[g].y), bfh(bia[g].y)};
                float o[4];
#pragma unroll
                for (int e = 0; e < 4; ++e) {
                    const float a = acc[0][4 * g + e] + bs[e];
                    const float z = a * fmaf(a * a, 0.044715f, 1.0f) * (-2.0f * 0.7978845608028654f * 1.4426950408889634f);
                    o[e] = a * __builtin_amdgcn_rcpf(1.0f + __builtin_amdgcn_exp2f(z));
                }
                uf[g >> 1][(g & 1) * 2] = pk(o[0], o[1]); uf[g >> 1][(g & 1) * 2 + 1] = pk(o[2], o[3]);
            }
            const int uo = WS_U + ((2 * md + (wave >> 1)) * 88 + ft * 8 + kq * 2) * 2048 + (wave & 1) * 1024;
            __builtin_amdgcn_raw_buffer_store_b128(uf[0], rws, lane * 16, uo, SC1);
            __builtin_amdgcn_raw_buffer_store_b128(uf[1], rws, lane * 16, uo + 2048, SC1);
            waitv<0>();
            if (lds_last(L + L_CNT + C_PUB, 4, lane)) {
                lds_wait<0>(L + L_CNT + C_SEEN, 8);
                set_flag(rws, WS_FLU + (md * 44 + ft * 4 + kq) * FLS, TLO, THI, lane);
            }
            STAMP(10);
            lds_wait<0>(L + L_CNT + C_RING, 4);
            ww.cur = ww.lo + (rot + RSL >= ns ? rot + RSL - ns : rot + RSL) * ww.step;
        } else {
            if (role == 1) {
                const rsrc_t rx = mk((const char*)p.x + ct * 128 * 1024, 128 * 1024);
                const rsrc_t rbr = mk((const char*)p.bre + (pg * 128 + wave * 32) * 1024, 32 * 1024);
                const rsrc_t rbi = mk((const char*)p.bim + (pg * 128 + wave * 32) * 1024, 32 * 1024);
                const int tok0 = 64 * hh + 32 * ((lane >> 3) & 1) + 8 * wave + cb;
                const unsigned c0 = ((lane & 7) ^ (lane >> 4)) * 16;
                const unsigned va0 = tok0 * 1024 + c0, va1 = va0 ^ 64, vb0 = (lane >> 3) * 1024 + c0, vb1 = vb0 ^ 64;
                int kt = (ct + 2 * pg) & 7;
                unsigned base = Lb;
#pragma unroll 1
                for (int n = 0; n < 8; ++n) {
                    const unsigned ko = kt * 128;
#pragma unroll
                    for (int i = 0; i < 4; ++i) glds<true>(base + (4 * wave + i) * 1024, (i & 1) ? va1 : va0, rx, ko + i * 2048);
#pragma unroll
                    for (int i = 0; i < 4; ++i) glds<false>(base + 16384 + (8 * wave + i) * 1024, (i & 1) ? vb1 : vb0, rbr, ko + i * 8192);
                    if (n > 0) { waitv<8>(); bar(); }
#pragma unroll
                    for (int i = 0; i < 4; ++i) glds<false>(base + 16384 + (8 * wave + 4 + i) * 1024, (i & 1) ? vb1 : vb0, rbi, ko + i * 8192);
                    kt = (kt + 1) & 7; base = base == Lb + 98304 ? Lb : base + 49152;
                }
                waitv<0>(); bar();
                bar();
                STAMP(1);
                GET_TAG();
            }
            epi_load(ep, p, rws, tt, dt, tid);
#pragma unroll 1
            for (int i = 0; i < RSL; ++i) { w_issue(ringaddr + i * 2048, w2v, rw2, ww); waitv<THR>(); }
            waitv<0>();
        }
        LAS char* ring = L + L_RING + wave * (RSL * 2048);
        const unsigned tr0 = (4 * hh + q4) * 128 + (4 * (q4 >> 1) + 2 * cb + (p4 >> 1)) * 16 + 8 * (p4 & 1), tr1 = tr0 ^ 64;
        u32x4 wp[RSL][2];
#pragma unroll
        for (int i = 0; i < RSL; ++i) {
            wp[i][0] = trread2(ring + i * 2048 + tr0, ring + i * 2048 + tr0 + 1024);
            wp[i][1] = trread2(ring + i * 2048 + tr1, ring + i * 2048 + tr1 + 1024);
        }
        waitl();
#pragma unroll 1
        for (int i = 0; i < RSL; ++i) w_issue(ringaddr + i * 2048, w2v, rw2, ww);
        STAMP(4);
        {
            const int n0 = s0 >> 1, nf = ((s0 + ns - 1) >> 1) - n0 + 1;
            poll_flags(rws, WS_FLU + ((dt >> 1) * 44 + n0 + (lane < nf ? lane : 0)) * FLS, TLO, THI);
        }
        waitv<0>();
        STAMP(5);
        constexpr int DA = 6;
        u32x4 af[DA][2], wf[2][2];
        Walk aw(WS_U + (dt * 88 + s0) * 2048, 2048, ns, rot);
#pragma unroll
        for (int i = 0; i < DA; ++i) {
            const unsigned o = aw.next();
            af[i][0] = __builtin_amdgcn_raw_buffer_load_b128(rws, lane * 16, o, SC1);
            af[i][1] = __builtin_amdgcn_raw_buffer_load_b128(rws, lane * 16 + 1024, o, SC1);
        }
#pragma unroll
        for (int a = 0; a < 4; ++a) acc[a] = (f32x16)(0.f);
        SCHED();
#pragma unroll
        for (int i = 0; i < 22; ++i) {
            if (i + 1 < 22 && i + 1 >= RSL) {
                LAS char* sl = ring + (i + 1 - RSL) * 2048;
                wf[(i + 1) & 1][0] = trread2(sl + tr0, sl + tr0 + 1024);
                wf[(i + 1) & 1][1] = trread2(sl + tr1, sl + tr1 + 1024);
            }
            SCHED();
            if (i + 1 < 22 || wave < 2) {
#pragma unroll
                for (int a = 0; a < 4; ++a) acc[a] = mfma(af[i % DA][a >> 1], i < RSL ? wp[i < RSL ? i : 0][a & 1] : wf[i & 1][a & 1], acc[a]);
            }
            SCHED();
            if (i + DA < 22) {
                const unsigned o = aw.next();
                af[i % DA][0] = __builtin_amdgcn_raw_buffer_load_b128(rws, lane * 16, o, SC1);
                af[i % DA][1] = __builtin_amdgcn_raw_buffer_load_b128(rws, lane * 16 + 1024, o, SC1);
            }
            SCHED();
        }
        STAMP(6);
        if (b == 161 && wave == 0) {
#pragma unroll 1
            for (int i = 0; i < 4; ++i) poll_flags(rws, WS_FLU + (64 * i + lane) * FLS, TLO, THI);
            set_flag(rws, WS_EPOCH, TLO, THI, lane);
        }
    } else {
        __builtin_amdgcn_s_setprio(2);
        const int v = wave - 4, g = lane >> 4, li = lane & 15;
        LAS char* Q = L + L_AUX + v * 16384;
        if (j < 16) {
            const int lg = xg * 16 + j;
            f32x2 f[16];
#pragma unroll
            for (int c = 0; c < 4; ++c)
#pragma unroll
                for (int e = 0; e < 4; ++e) { f[4 * c + e].x = bfl(xr[c][e]); f[4 * c + e].y = bfh(xr[c][e]); }
            STAMP(7);
            f32x2 s2 = f[0];
#pragma unroll
            for (int e = 1; e < 16; ++e) s2 += f[e];
            const float m = row_sum(s2.x + s2.y) * (1.0f / D);
            f32x2 q2 = {0.f, 0.f};
#pragma unroll
            for (int e = 0; e < 16; ++e) { f[e] -= m; q2 += f[e] * f[e]; }
            const float var = row_sum(q2.x + q2.y) * (1.0f / D);
            const float ra = __builtin_amdgcn_rsqf(var + EPS);
            const float sc = ra * __builtin_amdgcn_rsqf(var * ra * ra + EPS);
#pragma unroll
            for (int c = 0; c < 4; ++c) {
                u32x4 o;
#pragma unroll
                for (int e = 0; e < 4; ++e) { const f32x2 t = f[4 * c + e] * sc; o[e] = pk(t.x, t.y); }
                *(LAS u32x4*)(L + L_LN + (4 * v + g) * 1024 + (((li + 16 * c) ^ (g << 2)) * 16)) = o;
            }
            lds_arrive(L + L_CNT + C_LN, lane);
            GET_TAG();
            if (li == 0) {
                u32x4 sv = {asu(m), TLO, asu(ra), TLO};
                __builtin_amdgcn_raw_buffer_store_b128(sv, rws, g * 16, WS_STATS + (lg * 16 + 4 * v) * 16, SC1);
            }
            STAMP(10);
            lds_wait<0>(L + L_CNT + C_LN, 4);
            STAMP(4);
#pragma unroll
            for (int i = 0; i < 4; ++i) {
                LAS char* a = L + L_LN + (4 * hh + q4) * 1024 + (16 * v + 4 * (i ^ q4) + 2 * cb + (p4 >> 1)) * 16 + 8 * (p4 & 1);
                const u32x4 fr = trread2(a, a + 8192);
                __builtin_amdgcn_raw_buffer_store_b128(fr, rws, lane * 16, WS_H2 + ((2 * v + (i >> 1)) * 128 + lg) * 2048 + (i & 1) * 1024, SC1);
            }
            STAMP(1);
            waitv<0>();
            set_flag(rws, WS_FLLN + (v * 128 + lg) * FLS, TLO, THI, lane);
            STAMP(2);
        } else if (j < 24) {
            if (role == 0) { GET_TAG(); }
        } else {
            GET_TAG();
            const int ch = pg * 128 + v * 32 + l31;
            unsigned rd[4];
#pragma unroll
            for (int ks = 0; ks < 4; ++ks) rd[ks] = l31 * 128 + (((2 * ks + hh) ^ ((lane >> 1) & 7)) * 16);
            f32x16 ac[4][2];
#pragma unroll
            for (int a = 0; a < 8; ++a) { ac[a >> 1][a & 1] = (f32x16)(0.f); asm volatile("" : "+v"(ac[a >> 1][a & 1])); }
            u32x4 fr[2][6];
            const unsigned bo = 16384 + v * 8192;
            u32x4 sg0, sg1, cg0, cg1;
            bar();
            STAMP(1); CYC(cyc0);
#pragma unroll
            for (int k = 0; k < 6; ++k) fr[0][k] = *(LAS u32x4*)(L + rd[0] + (k < 2 ? bo + k * 4096 : (k - 2) * 4096));
            unsigned buf = 0;
#pragma unroll 1
            for (int n = 0; n < 8; ++n) {
#pragma unroll
                for (int ks = 0; ks < 4; ++ks) {
                    if (ks == 3) { bar(); buf = buf == 98304 ? 0 : buf + 49152; }
                    LAS char* A = L + buf + rd[(ks + 1) & 3];
                    LAS char* B = A + bo;
                    SCHED();
#pragma unroll
                    for (int k = 0; k < 8; ++k) {
                        ac[k >> 1][k & 1] = mfma(fr[ks & 1][2 + (k >> 1)], fr[ks & 1][k & 1], ac[k >> 1][k & 1]);
                        if (k < 6) fr[(ks + 1) & 1][k] = *(LAS u32x4*)(k < 2 ? B + k * 4096 : A + (k - 2) * 4096);
                        SCHED();
                    }
                }
                if (n == 6) {
                    sg0 = __builtin_amdgcn_raw_buffer_load_b128(rws, lane * 16, WS_STATS + ct * 2048, SC1);
                    sg1 = __builtin_amdgcn_raw_buffer_load_b128(rws, lane * 16 + 1024, WS_STATS + ct * 2048, SC1);
                    cg0 = __builtin_amdgcn_raw_buffer_load_b128(rws, ch * 32, WS_CH, SC1);
                    cg1 = __builtin_amdgcn_raw_buffer_load_b128(rws, ch * 32 + 16, WS_CH, SC1);
                }
            }
            STAMP(2); CYC(cyc1);
#pragma unroll 1
            for (unsigned sp = 0; sp < SPIN_CAP; ++sp) {
                const unsigned bad = (sg0.y ^ tlo) | (sg0.w ^ tlo) | (sg1.y ^ tlo) | (sg1.w ^ tlo) | (cg0.y ^ tlo) | (cg0.w ^ tlo) | (cg1.y ^ tlo) | (cg1.w ^ tlo);
                if (__builtin_amdgcn_ballot_w64(bad != 0) == 0) break;
                __builtin_amdgcn_s_sleep(1);
                asm volatile("" ::: "memory");
                sg0 = __builtin_amdgcn_raw_buffer_load_b128(rws, lane * 16, WS_STATS + ct * 2048, SC1);
                sg1 = __builtin_amdgcn_raw_buffer_load_b128(rws, lane * 16 + 1024, WS_STATS + ct * 2048, SC1);
                cg0 = __builtin_amdgcn_raw_buffer_load_b128(rws, ch * 32, WS_CH, SC1);
                cg1 = __builtin_amdgcn_raw_buffer_load_b128(rws, ch * 32 + 16, WS_CH, SC1);
            }
            {
                LAS float* T = (LAS float*)(Q + l31 * 16 + hh * 4);
                T[0] = asf(sg0.z); T[2] = -asf(sg0.x) * asf(sg0.z);
                T[128] = asf(sg1.z); T[130] = -asf(sg1.x) * asf(sg1.z);
            }
            waitl();
            const f32x2 lam = {asf(cg0.x), asf(cg0.z)};
            const float sbr = asf(cg1.x), sbi = asf(cg1.z);
            f32x2 er = {0.f, 0.f}, ei = {0.f, 0.f};
            f32x4 ab[2][8];
#pragma unroll
            for (int i = 0; i < 8; ++i) ab[0][i] = *(LAS f32x4*)(Q + hh * 512 + i * 16);
#pragma unroll
            for (int m = 0; m < 4; ++m) {
                if (m < 3) {
#pragma unroll
                    for (int i = 0; i < 8; ++i) ab[(m + 1) & 1][i] = *(LAS f32x4*)(Q + hh * 512 + (8 * m + 8 + i) * 16);
                }
                SCHED();
#pragma unroll
                for (int i = 0; i < 8; ++i) {
                    const f32x2 a2 = {ab[m & 1][i].x, ab[m & 1][i].y}, b2 = {ab[m & 1][i].z, ab[m & 1][i].w};
                    const int r = 2 * i;
                    f32x2 vr = {ac[m][0][r], ac[m][0][r + 1]}, vi = {ac[m][1][r], ac[m][1][r + 1]};
                    vr = a2 * vr + b2 * sbr; vi = a2 * vi + b2 * sbi;
                    ac[m][0][r] = vr.x; ac[m][0][r + 1] = vr.y; ac[m][1][r] = vi.x; ac[m][1][r + 1] = vi.y;
                    const f32x2 nr = lam.x * er + (vr - lam.y * ei), ni = lam.x * ei + (vi + lam.y * er);
                    er = nr; ei = ni;
                }
                SCHED();
            }
            f32x2 l32 = lam;
#pragma unroll
            for (int i = 0; i < 5; ++i) l32 = cmul(l32, l32);
            const f32x2 l64 = cmul(l32, l32), l128 = cmul(l64, l64), l256 = cmul(l128, l128);
            const f32x2 e0 = {er.x, ei.x}, e1 = {er.y, ei.y};
            const f32x2 eh = cfma(l32, e0, e1);
            const f32x2 eo = {other_half(eh.x), other_half(eh.y)};
            if (hh == 0) {
                const f32x2 E = cfma(l64, eh, eo);
                u32x4 ge = {asu(E.x), tlo, asu(E.y), tlo};
                __builtin_amdgcn_raw_buffer_store_b128(ge, rws, ch * 16, WS_E + ct * (P * 16), SC1);
            }
            STAMP(3);
            f32x2 H = {0.f, 0.f};
            {
                u32x4 ge[8];
#pragma unroll 1
                for (unsigned sp = 0; sp < SPIN_CAP; ++sp) {
                    asm volatile("" ::: "memory");
                    bool bad = false;
#pragma unroll
                    for (int qq = 0; qq < 8; ++qq) {
                        const int jc = ct - 1 - hh - 2 * qq;
                        ge[qq] = __builtin_amdgcn_raw_buffer_load_b128(rws, jc < 0 ? 0x40000000 : jc * (P * 16) + ch * 16, WS_E, SC1);
                        bad |= jc >= 0 && (ge[qq].y != tlo || ge[qq].w != tlo);
                    }
                    if (__builtin_amdgcn_ballot_w64(bad) == 0) break;
                }
#pragma unroll
                for (int qq = 7; qq >= 0; --qq) { const f32x2 ev = {asf(ge[qq].x), asf(ge[qq].z)}; H = cfma(l256, H, ev); }
                if (hh) H = cmul(H, l128);
                H.x += other_half(H.x); H.y += other_half(H.y);
            }
            STAMP(4);
            if (hh) H = cfma(l64, H, eo);
            const f32x2 H1 = cfma(l32, H, e0);
            f32x2 hr = {H.x, H1.x}, hi = {H.y, H1.y};
            const int fw = ((l31 & 3) << 2) | ((l31 >> 1) & 3);
#pragma unroll
            for (int m = 0; m < 4; ++m) {
                u32x4 o[4];
#pragma unroll
                for (int i2 = 0; i2 < 4; ++i2) {
                    const int r = 4 * i2;
                    const f32x2 vr0 = {ac[m][0][r], ac[m][0][r + 1]}, vi0 = {ac[m][1][r], ac[m][1][r + 1]};
                    const f32x2 vr1 = {ac[m][0][r + 2], ac[m][0][r + 3]}, vi1 = {ac[m][1][r + 2], ac[m][1][r + 3]};
                    const f32x2 ar = lam.x * hr + (vr0 - lam.y * hi), ai = lam.x * hi + (vi0 + lam.y * hr);
                    hr = lam.x * ar + (vr1 - lam.y * ai); hi = lam.x * ai + (vi1 + lam.y * ar);
                    o[0][i2] = pk(ar.x, hr.x); o[1][i2] = pk(ar.y, hr.y); o[2][i2] = pk(ai.x, hi.x); o[3][i2] = pk(ai.y, hi.y);
                }
#pragma unroll
                for (int u = 0; u < 4; ++u)
                    *(LAS u32x4*)(Q + (u >> 1) * 8192 + l31 * 256 + (((8 * hh + 4 * (u & 1) + m) ^ fw) * 16)) = o[u];
            }
            waitl();
            {
                const int r0 = 8 * hh + q4, f0 = (q4 << 2) | ((r0 >> 1) & 3), f1 = (q4 << 2) | (((r0 + 4) >> 1) & 3);
                const int ho = WS_H + (2 * ct * 64 + pg * 16 + v * 4) * 2048;
                u32x4 fo[16];
#pragma unroll
                for (int tq = 0; tq < 4; ++tq) {
                    const int c = 4 * tq + 2 * cb + (p4 >> 1);
                    LAS char* a0 = Q + r0 * 256 + ((c ^ f0) * 16) + 8 * (p4 & 1);
                    LAS char* a1 = Q + (r0 + 4) * 256 + ((c ^ f1) * 16) + 8 * (p4 & 1);
#pragma unroll
                    for (int kk = 0; kk < 4; ++kk) fo[tq * 4 + kk] = trread2(a0 + kk * 4096, a1 + kk * 4096);
                }
                SCHED();
#pragma unroll
                for (int k = 0; k < 16; ++k)
                    __builtin_amdgcn_raw_buffer_store_b128(fo[k], rws, lane * 16 + ((k >> 2) & 1) * 1024, ho + ((k >> 3) * 64 + (k & 3)) * 2048, SC1);
            }
            STAMP(7);
            waitv<0>();
            if (lds_last(L + L_CNT + C_PUB, 4, lane)) {
                lds_wait<0>(L + L_CNT + C_SEEN, 8);
                set_flag(rws, WS_FLH + (ct * 4 + pg) * FLS, tlo, THI, lane);
            }
            STAMP(10);
        }
        __builtin_amdgcn_s_setprio(0);
        constexpr int CP = 14, HD = 5;
        if (role == 0) lds_wait<2>(L + L_CNT + C_SEEN, 8);
        epi_load(ep, p, rws, tt, dt, tid);
        if (role == 0) lds_wait<2>(L + L_CNT + C_H2, 4);
        poll_flags(rws, WS_FLC + (2 * dt + (lane & 1)) * FLS, TLO, THI);
        u32x4 cf[CP][2], hf[HD][2];
        const unsigned co = WS_CT + (dt * 64 + v * 16) * 2048, ho = WS_H + (tt * 64 + v * 16) * 2048;
#pragma unroll
        for (int i = 0; i < CP; ++i) {
            cf[i][0] = __builtin_amdgcn_raw_buffer_load_b128(rws, lane * 16, co + i * 2048, SC1);
            cf[i][1] = __builtin_amdgcn_raw_buffer_load_b128(rws, lane * 16 + 1024, co + i * 2048, SC1);
            waitv<THR>();
        }
        SCHED();
        STAMP(11);
        if (role == 0) {
            Walk ww(s0 * 16 * (S * 2) + tt * 128, 16 * S * 2, ns, rot);
            lds_wait<2>(L + L_CNT + C_G3, 4);
#pragma unroll 1
            for (int i = 0; i < RSL; ++i) { w_issue(ringaddr + i * 2048, w2v, rw2, ww); waitv<THR>(); }
            waitv<0>();
            lds_arrive(L + L_CNT + C_RING, lane);
            STAMP(3);
        }
        poll_flags(rws, WS_FLH + ((tt >> 1) * 4 + v) * FLS, TLO, THI);
        STAMP(5);
#pragma unroll
        for (int i = 0; i < HD; ++i) {
            hf[i][0] = __builtin_amdgcn_raw_buffer_load_b128(rws, lane * 16, ho + i * 2048, SC1);
            hf[i][1] = __builtin_amdgcn_raw_buffer_load_b128(rws, lane * 16 + 1024, ho + i * 2048, SC1);
        }
#pragma unroll
        for (int a = 0; a < 4; ++a) acc[a] = (f32x16)(0.f);
        SCHED();
#pragma unroll
        for (int i = 0; i < 16; ++i) {
#pragma unroll
            for (int a = 0; a < 4; ++a) acc[a] = mfma(cf[i % CP][a >> 1], hf[i % HD][a & 1], acc[a]);
            SCHED();
            if (i + CP < 16) {
                cf[i % CP][0] = __builtin_amdgcn_raw_buffer_load_b128(rws, lane * 16, co + (i + CP) * 2048, SC1);
                cf[i % CP][1] = __builtin_amdgcn_raw_buffer_load_b128(rws, lane * 16 + 1024, co + (i + CP) * 2048, SC1);
            }
            if (i + HD < 16) {
                hf[i % HD][0] = __builtin_amdgcn_raw_buffer_load_b128(rws, lane * 16, ho + (i + HD) * 2048, SC1);
                hf[i % HD][1] = __builtin_amdgcn_raw_buffer_load_b128(rws, lane * 16 + 1024, ho + (i + HD) * 2048, SC1);
            }
            SCHED();
        }
        STAMP(6);
        if (role == 0) lds_wait<0>(L + L_CNT + C_RING, 4);
    }
    {
        LAS char* I = L + (wave < 4 ? L_RING + wave * (RSL * 2048) : L_AUX + (wave - 4) * 16384);
        const int sw = (l31 & 7) ^ (((l31 >> 1) & 1) << 3);
#pragma unroll
        for (int a = 0; a < 4; ++a)
#pragma unroll
            for (int g = 0; g < 4; ++g) {
                f32x4 v = {acc[a][4 * g], acc[a][4 * g + 1], acc[a][4 * g + 2], acc[a][4 * g + 3]};
                *(LAS f32x4*)(I + ((a & 1) * 32 + l31) * 256 + ((((a >> 1) * 8 + 2 * g + hh) ^ sw) * 16)) = v;
            }
    }
    bar();
    STAMP(8);
    {
        const int t = tid >> 3, sw = (t & 7) ^ (((t >> 1) & 1) << 3);
        const int et = tt * 64 + t, ed = dt * 64 + (tid & 7) * 4;
        if (ep.sg.y != tlo || ep.sg.w != tlo) ep.sg = __builtin_amdgcn_raw_buffer_load_b128(rws, et * 16, WS_STATS, SC1);
        const float mean = asf(ep.sg.x), rstd = asf(ep.sg.z);
#pragma unroll
        for (int h2 = 0; h2 < 2; ++h2) {
            LAS char* R = L + t * 256 + ((((tid & 7) + 8 * h2) ^ sw) * 16);
            f32x4 y2 = *(LAS f32x4*)(R + L_RING), y = *(LAS f32x4*)(R + L_AUX);
#pragma unroll
            for (int w = 1; w < 4; ++w) { y2 += *(LAS f32x4*)(R + L_RING + w * (RSL * 2048)); y += *(LAS f32x4*)(R + L_AUX + w * 16384); }
            const u32x2 xx = h2 ? ep.x1 : ep.x0, kk = h2 ? ep.k1 : ep.k0;
            const float xf[4] = {bfl(xx.x), bfh(xx.x), bfl(xx.y), bfh(xx.y)};
            const float kf[4] = {bfl(kk.x), bfh(kk.x), bfl(kk.y), bfh(kk.y)};
            f32x4 o;
#pragma unroll
            for (int e = 0; e < 4; ++e) {
                const float z = y2[e] + ep.b2;
                const float g = z * __builtin_amdgcn_rcpf(1.0f + __builtin_amdgcn_exp2f(z * -1.4426950408889634f));
                const float s = fmaf((xf[e] - mean) * rstd, kf[e], y[e]);
                o[e] = fmaf(s, g, xf[e]);
            }
            *(GAS f32x4*)((GAS char*)p.out + (et * D + ed + h2 * 32) * 4) = o;
        }
    }
    STAMP(9);
    if (role == PROLE && (wave >> 2) == PGRP && j < PJ && pr1) {
        const unsigned long long te = __builtin_amdgcn_s_memrealtime() + 4 * (pr1 - pr0);
        while (__builtin_amdgcn_s_memrealtime() < te) __builtin_amdgcn_s_sleep(1);
    }
}

extern "C" void kernel_launch(void* const* d_in, const int* in_sizes, int n_in, void* d_out, int out_size, void* d_ws, size_t ws_size,
                              hipStream_t stream) {
    Params p;
    p.x = d_in[0]; p.lre = d_in[1]; p.lim = d_in[2]; p.bre = d_in[3]; p.bim = d_in[4]; p.cre = d_in[5]; p.cim = d_in[6];
    p.ldt = d_in[7]; p.dsk = d_in[8]; p.w1 = d_in[10]; p.b1 = d_in[11]; p.w2 = d_in[12]; p.b2 = d_in[13];
    p.out = d_out; p.ws = d_ws;
    fused_kernel<<<256, 512, 0, stream>>>(p);
}
```

```cpp
#define XNT false
#define W1NT false
#define W2NT false
#include <hip/hip_runtime.h>
#include <hip/hip_bf16.h>
#include <stdint.h>
#include <utility>

#define LAS __attribute__((address_space(3)))
#define GAS __attribute__((address_space(1)))
#define CAS __attribute__((address_space(4)))
#define DI __device__ __forceinline__
typedef short s16x4 __attribute__((ext_vector_type(4)));
typedef short s16x8 __attribute__((ext_vector_type(8)));
typedef __bf16 bf16x8 __attribute__((ext_vector_type(8)));
typedef __bf16 bf16x2 __attribute__((ext_vector_type(2)));
typedef float f32x2 __attribute__((ext_vector_type(2)));
typedef float f32x4 __attribute__((ext_vector_type(4)));
typedef float f32x16 __attribute__((ext_vector_type(16)));
typedef unsigned u32x2 __attribute__((ext_vector_type(2)));
typedef unsigned u32x4 __attribute__((ext_vector_type(4)));
typedef __amdgpu_buffer_rsrc_t rsrc_t;

constexpr int S = 2048, D = 512, P = 512, F = 1376;
constexpr float EPS = 1e-5f;
constexpr int SC1 = 16;
constexpr int FLS = 128;
constexpr int WS_EPOCH = 0;
constexpr int WS_FLU = 4096;
constexpr int WS_FLH = WS_FLU + 176 * FLS;
constexpr int WS_FLC = WS_FLH + 64 * FLS;
constexpr int WS_FLLN = WS_FLC + 16 * FLS;
constexpr int WS_STATS = 256 * 1024;
constexpr int WS_CH = WS_STATS + S * 16;
constexpr int WS_E = WS_CH + P * 32;
constexpr int WS_H2 = 1 << 20;
constexpr int WS_U = WS_H2 + 8 * 128 * 2048;
constexpr int WS_CT = WS_U + 8 * 88 * 2048;
constexpr int WS_H = WS_CT + 8 * 64 * 2048;
constexpr int WS_PX = WS_H + 32 * 64 * 2048;
constexpr int WS_END = WS_PX + 44 * 16 * 9216;
constexpr int WS_STAMP = 32 << 20;
static_assert(WS_FLLN + 512 * FLS <= WS_STATS && WS_E + 16 * P * 16 <= WS_H2 && WS_END <= WS_STAMP, "ws");
constexpr int RSL = 11;
constexpr int L_RING = 0;
constexpr int L_AUX = 4 * RSL * 2048;
constexpr int L_LN = 131072;
constexpr int L_DUMMY = L_AUX + 65536;
constexpr int L_CNT = L_DUMMY + 4096;
constexpr int LDS_BYTES = 163840;
static_assert(L_CNT + 64 <= LDS_BYTES && 3 * 49152 <= L_DUMMY && L_LN + 16384 <= L_DUMMY, "lds");
constexpr int C_SEEN = 0, C_LN = 4, C_LNST = 8, C_G3 = 12, C_RING = 16, C_PUB = 20, C_PUB2 = 24, C_GO = 28, C_XL = 32, C_H1 = 36, C_H2 = 40;
constexpr unsigned SPIN_CAP = 1u << 17;

struct Params {
    const void* x; const void* lre; const void* lim; const void* bre; const void* bim; const void* cre; const void* cim;
    const void* ldt; const void* dsk; const void* w1; const void* b1; const void* w2; const void* b2;
    void* out; void* ws;
};

DI rsrc_t mk(const void* p, int bytes) { return __builtin_amdgcn_make_buffer_rsrc((void*)p, 0, bytes, 0x00020000); }
DI void st128(u32x4 v, rsrc_t rs, int voff, int soff, int) { __builtin_amdgcn_raw_buffer_store_b128(v, rs, voff + soff, 0, SC1); }
DI float asf(unsigned v) { return __builtin_bit_cast(float, v); }
DI unsigned asu(float v) { return __builtin_bit_cast(unsigned, v); }
DI bf16x2 asb2(unsigned v) { return __builtin_bit_cast(bf16x2, v); }
DI float bfl(unsigned v) { return __builtin_bit_cast(float, v << 16); }
DI float bfh(unsigned v) { return __builtin_bit_cast(float, v & 0xffff0000u); }
DI unsigned pk(float lo, float hi) { f32x2 v = {lo, hi}; return __builtin_bit_cast(unsigned, __builtin_convertvector(v, bf16x2)); }
DI float ldbf(const void* p, int i) { return bfl(((const GAS unsigned short*)p)[i]); }
DI f32x16 mfma(u32x4 a, u32x4 b, f32x16 c) {
    return __builtin_amdgcn_mfma_f32_32x32x16_bf16(__builtin_bit_cast(bf16x8, a), __builtin_bit_cast(bf16x8, b), c, 0, 0, 0);
}
DI u32x4 trread2(LAS char* lo, LAS char* hi) {
    s16x4 a = __builtin_amdgcn_ds_read_tr16_b64_v4i16((LAS s16x4*)lo);
    s16x4 b = __builtin_amdgcn_ds_read_tr16_b64_v4i16((LAS s16x4*)hi);
    s16x8 r = __builtin_shufflevector(a, b, 0, 1, 2, 3, 4, 5, 6, 7);
    return __builtin_bit_cast(u32x4, r);
}
template <bool NTL> DI void glds(unsigned ldsaddr, unsigned voff, rsrc_t rs, unsigned soff) {
    if (NTL) asm volatile("s_mov_b32 m0, %0\n\ts_nop 3\n\tbuffer_load_dwordx4 %1, %2, %3 offen nt lds" ::"s"(ldsaddr), "v"(voff), "s"(rs), "s"(soff) : "memory");
    else     asm volatile("s_mov_b32 m0, %0\n\ts_nop 3\n\tbuffer_load_dwordx4 %1, %2, %3 offen lds" ::"s"(ldsaddr), "v"(voff), "s"(rs), "s"(soff) : "memory");
}
template <int N> DI void waitv() { asm volatile("s_waitcnt vmcnt(%0)" ::"i"(N) : "memory"); }
DI void waitl() { asm volatile("s_waitcnt lgkmcnt(0)" ::: "memory"); }
DI void bar() { asm volatile("s_waitcnt lgkmcnt(0)\n\ts_barrier" ::: "memory"); }
#define SCHED() __builtin_amdgcn_sched_barrier(0)
template <int CTRL> DI float dpp(float v) { return asf((unsigned)__builtin_amdgcn_update_dpp(0, (int)asu(v), CTRL, 0xf, 0xf, true)); }
DI float row_sum(float v) {
    v += dpp<0x128>(v); v += dpp<0x124>(v); v += dpp<0x122>(v); v += dpp<0x121>(v);
    return v;
}
DI float other_half(float v) { return __shfl_xor(v, 32); }
DI void poll_flags(rsrc_t ws, int o, unsigned tlo, unsigned thi) {
#pragma unroll 1
    for (unsigned sp = 0; sp < SPIN_CAP; ++sp) {
        asm volatile("" ::: "memory");
        u32x2 v = __builtin_amdgcn_raw_buffer_load_b64(ws, o, 0, SC1);
        if (__builtin_amdgcn_ballot_w64(v.x != tlo || v.y != thi) == 0) break;
        __builtin_amdgcn_s_sleep(1);
    }
    asm volatile("" ::: "memory");
}
DI void lds_add(LAS char* cnt, int lane) {
    asm volatile("ds_add_u32 %0, %1" ::"v"((unsigned)(size_t)cnt + (lane ? 2044 + lane * 4 : 0)), "v"(1u) : "memory");
}
DI void lds_arrive(LAS char* cnt, int lane) { waitl(); lds_add(cnt, lane); }
template <int SLEEP> DI void lds_wait(LAS char* cnt, unsigned n) {
#pragma unroll 1
    for (unsigned sp = 0; sp < (SPIN_CAP << 4); ++sp) {
        asm volatile("" ::: "memory");
        if ((unsigned)__builtin_amdgcn_readfirstlane(*(volatile LAS unsigned*)cnt) >= n) break;
        if (SLEEP) __builtin_amdgcn_s_sleep(SLEEP);
    }
    asm volatile("" ::: "memory");
}
DI bool lds_last(LAS char* cnt, unsigned n, int lane) {
    unsigned old = 0;
    if (lane == 0) old = __hip_atomic_fetch_add((LAS unsigned*)cnt, 1u, __ATOMIC_RELAXED, __HIP_MEMORY_SCOPE_WORKGROUP);
    asm volatile("" ::: "memory");
    return (unsigned)__builtin_amdgcn_readfirstlane(old) == n - 1;
}
DI void set_flag(rsrc_t ws, int off, unsigned tlo, unsigned thi, int lane) {
    if (lane == 0) { u32x2 v = {tlo, thi}; __builtin_amdgcn_raw_buffer_store_b64(v, ws, off, 0, SC1); }
}
DI f32x2 cmul(f32x2 a, f32x2 b) { f32x2 r; r.x = a.x * b.x - a.y * b.y; r.y = a.x * b.y + a.y * b.x; return r; }
DI f32x2 cfma(f32x2 a, f32x2 b, f32x2 c) {
    f32x2 r; r.x = fmaf(a.x, b.x, fmaf(-a.y, b.y, c.x)); r.y = fmaf(a.x, b.y, fmaf(a.y, b.x, c.y)); return r;
}
DI void lam_of(unsigned r_dt, unsigned r_re, unsigned r_im, f32x2& lam, f32x2& Lam) {
    const float dt0 = expf(bfl(r_dt));
    const float dt = bfl(pk(dt0, dt0));
    Lam.x = bfl(r_re); Lam.y = bfl(r_im);
    const float e = expf(dt * Lam.x);
    float sn, cs; sincosf(dt * Lam.y, &sn, &cs);
    lam.x = e * cs; lam.y = e * sn;
}

#define STAMP(k) do { } while (0)
#define CYC(v) do { } while (0)

#define STHR 5
#define BNT false
#define CAUX 0
#define CP1 3
#define NH2 47
#define THR 6
#define CODE_LEN 33792
template <class Fn, int... I> DI void sfor_impl(Fn&& f, std::integer_sequence<int, I...>) { (f(std::integral_constant<int, I>{}), ...); }
template <int N, class Fn> DI void sfor(Fn&& f) { sfor_impl(f, std::make_integer_sequence<int, N>{}); }
constexpr int tile_of(int n) { return n < 64 ? 3 - (n & 1) : (n < 96 ? 1 : 0); }
constexpr int step_of(int n) { return n < 64 ? n >> 1 : (n & 31); }
struct Walk {
    unsigned cur, lo, hi, step;
    DI Walk(unsigned base, unsigned st, int n, int rot) : cur(base + rot * st), lo(base), hi(base + n * st), step(st) {}
    DI unsigned next() { const unsigned r = cur; cur += step; cur = cur == hi ? lo : cur; return r; }
};
DI void w_issue(unsigned slotaddr, unsigned wv, rsrc_t rw, Walk& w) {
    const unsigned o = w.next();
    glds<false>(slotaddr, wv, rw, o);
    glds<false>(slotaddr + 1024, wv, rw, o + 8 * S * 2);
}
struct Epi { u32x2 x0, x1, k0, k1; u32x4 sg; float b2; };
DI void epi_load(Epi& e, const Params& p, rsrc_t rws, int tt, int dt, int tid) {
    const int et = tt * 64 + (tid >> 3), ed = dt * 64 + (tid & 7) * 4;
    e.x0 = *(const GAS u32x2*)((const GAS char*)p.x + (et * D + ed) * 2);
    e.x1 = *(const GAS u32x2*)((const GAS char*)p.x + (et * D + ed + 32) * 2);
    e.k0 = *(const GAS u32x2*)((const GAS char*)p.dsk + ed * 2);
    e.k1 = *(const GAS u32x2*)((const GAS char*)p.dsk + (ed + 32) * 2);
    e.b2 = ldbf(p.b2, et);
    e.sg = __builtin_amdgcn_raw_buffer_load_b128(rws, et * 16, WS_STATS, SC1);
}

__global__ void __launch_bounds__(512, 2) fused_kernel(Params p) {
    __shared__ __attribute__((aligned(16))) char smem[LDS_BYTES];
    LAS char* L = (LAS char*)smem;
    const unsigned Lb = (unsigned)(size_t)L;
    const int b = blockIdx.x, xg = b & 7, j = b >> 3;
    const int role = j >= 24 ? 1 : (((xg & 1) && j >= 20) ? 2 : 0);
    const int tid = threadIdx.x ^ (role == 1 ? 0 : 256), lane = tid & 63, wave = __builtin_amdgcn_readfirstlane(tid >> 6);
    STAMP(0);
    {
        unsigned long long pc;
        constexpr unsigned len = CODE_LEN;
        asm volatile("s_getpc_b64 %0" : "=s"(pc));
        const rsrc_t rc = mk((const void*)(pc & ~255ull), len);
        const unsigned piece = ((j - 16) * 8 + wave) * 1024;
        if (piece < len) glds<false>(Lb + L_DUMMY + (wave & 3) * 1024, lane * 16, rc, piece);
    }
    u32x4 xr[4];
    if (j < 16 && wave >= 4) {
#pragma unroll
        for (int c = 0; c < 4; ++c)
            xr[c] = __builtin_amdgcn_raw_buffer_load_b128(mk(p.x, S * D * 2), (xg * 256 + j * 16 + 4 * (wave - 4) + (lane >> 4)) * 1024 + ((lane & 15) + 16 * c) * 16, 0, XNT ? 2 : 0);
    }
    const rsrc_t rws = mk(p.ws, WS_END);
    const u32x2 eraw = __builtin_amdgcn_raw_buffer_load_b64(rws, WS_EPOCH, 0, SC1);
    const int hh = lane >> 5, l31 = lane & 31, cb = (lane >> 4) & 1, q4 = (lane >> 2) & 3, p4 = lane & 3;
    const int ct = xg * 2 + ((j >> 2) & 1), pg = j & 3;
    if (threadIdx.x < 16) *(LAS unsigned*)(L + L_CNT + threadIdx.x * 4) = 0u;
    const unsigned c0 = ((lane & 7) ^ (lane >> 4)) * 16, vb0 = (lane >> 3) * 1024 + c0, vb1 = vb0 ^ 64;
    const unsigned rd0 = l31 * 128 + ((hh ^ ((lane >> 1) & 7)) * 16);
    if (role == 1 && wave < 4) {
        const rsrc_t rx = mk((const char*)p.x + ct * 128 * 1024, 128 * 1024);
        const rsrc_t rbr = mk((const char*)p.bre + (pg * 128 + wave * 32) * 1024, 32 * 1024);
        const rsrc_t rbi = mk((const char*)p.bim + (pg * 128 + wave * 32) * 1024, 32 * 1024);
        const unsigned va0 = (64 * hh + 32 * ((lane >> 3) & 1) + 8 * wave + cb) * 1024 + c0, va1 = va0 ^ 64;
        int kt = (ct + 2 * pg) & 7;
        unsigned base = 0;
#pragma unroll 1
        for (int n = 0; n < 9; ++n) {
            const unsigned ko = kt * 128;
            if (n < 8) {
#pragma unroll
                for (int i = 0; i < 4; ++i) glds<false>(Lb + base + 16384 + (8 * wave + i) * 1024, (i & 1) ? vb1 : vb0, rbr, ko + i * 8192);
#pragma unroll
                for (int i = 0; i < 4; ++i) glds<false>(Lb + base + 16384 + (8 * wave + 4 + i) * 1024, (i & 1) ? vb1 : vb0, rbi, ko + i * 8192);
                if (n > 0) waitv<8>();
            } else waitv<0>();
            bar();
            if (n < 8) {
#pragma unroll
                for (int i = 0; i < 4; ++i) glds<XNT>(Lb + base + (4 * wave + i) * 1024, (i & 1) ? va1 : va0, rx, ko + i * 2048);
            }
            kt = (kt + 1) & 7; base = base == 98304 ? 0 : base + 49152;
        }
        bar();
        STAMP(1);
    } else bar();
    STAMP(12);
    if (role == 0 && wave < 4) {
        const rsrc_t rw1 = mk(p.w1, S * F * 2);
        const unsigned wv = (lane >> 4) * (F * 2) + ((lane & 15) ^ ((lane >> 4) << 2)) * 16;
        const unsigned wb = ((xg >> 1) * 512 + wave * 4) * (F * 2) + ((xg & 1) * 6 + (j >> 2)) * 256;
#pragma unroll 1
        for (int i = 0; i < 32; ++i) {
            const int ks = (i + 8 * j) & 31;
            glds<W1NT>(Lb + ks * 4096 + wave * 1024, wv, rw1, wb + ks * 16 * (F * 2));
            waitv<STHR>();
        }
    }
    if (j == 16 && wave >= 4) {
        const rsrc_t rv = wave == 4 ? mk(p.b1, F * 2) : (wave == 5 ? mk(p.dsk, D * 2) : mk((const char*)p.b2 + xg * 512, 512));
#pragma unroll 1
        for (int i = 0; i < 3; ++i) glds<false>(Lb + L_DUMMY + (wave & 3) * 1024, lane * 16, rv, i * 1024);
    }
    unsigned tlo, thi;
#define GET_TAG()                                                                     \
    tlo = __builtin_amdgcn_readfirstlane(eraw.x) + 1u;                                \
    thi = __builtin_amdgcn_readfirstlane(eraw.y);                                     \
    asm volatile("" ::"s"(tlo), "s"(thi));                                            \
    lds_add(L + L_CNT + C_SEEN, lane)
#define TLO tlo
#define THI thi
    const int tt = xg * 4 + (j >> 3), dt = j & 7;
    const rsrc_t rw2 = mk(p.w2, F * S * 2);
    const int wq = wave & 3;
    const int s0 = wq < 2 ? 22 * wq : 21 * wq + 2, ns = wq < 2 ? 22 : 21;
    const int rot = (dt * 21) >> 3;
    const unsigned w2v = (lane >> 3) * (S * 2) + ((lane & 7) ^ (cb << 2)) * 16;
    const unsigned ringaddr = Lb + L_RING + wq * (RSL * 2048);
    f32x16 acc[4];
    Epi ep;

    if (role == 2) {
        f32x2 lam;
        const int cid = (xg >> 1) * 4 + (j - 20);
        const int ch = wave * 64 + lane;
        const unsigned ko = wave < 4 ? L_RING + wave * (RSL * 2048) : L_AUX + wq * 16384;
        const rsrc_t rcr = mk((const char*)p.cre + cid * 32768, 32768), rci = mk((const char*)p.cim + cid * 32768, 32768);
        unsigned r_dt, r_re, r_im;
        asm volatile("s_nop 4\n\tglobal_load_ushort %0, %3, %4\n\tglobal_load_ushort %1, %3, %5\n\tglobal_load_ushort %2, %3, %6"
                     : "=&v"(r_dt), "=&v"(r_re), "=&v"(r_im) : "v"(ch * 2), "s"(p.ldt), "s"(p.lre), "s"(p.lim) : "memory");
#pragma unroll
        for (int i = 0; i < 8; ++i) glds<true>(Lb + ko + i * 1024, (i & 1) ? vb1 : vb0, i < 4 ? rcr : rci, wave * 128 + (i & 3) * 8192);
        unsigned q_dt, q_re, q_im;
        asm volatile("s_waitcnt vmcnt(8)\n\tv_mov_b32 %0, %3\n\tv_mov_b32 %1, %4\n\tv_mov_b32 %2, %5"
                     : "=&v"(q_dt), "=&v"(q_re), "=&v"(q_im) : "v"(r_dt), "v"(r_re), "v"(r_im) : "memory");
        f32x2 Lam;
        lam_of(q_dt, q_re, q_im, lam, Lam);
        GET_TAG();
        {
            if (cid == 0) {
                u32x4 lv = {asu(lam.x), TLO, asu(lam.y), TLO};
                st128(lv, rws, ch * 32, WS_CH, SC1);
            }
            const float inv = 2.0f / (Lam.x * Lam.x + Lam.y * Lam.y);
            LAS char* K = L + ko;
            *(LAS float*)(K + 8192 + lane * 4) = ((lam.x - 1.0f) * Lam.x + lam.y * Lam.y) * inv;
            *(LAS float*)(K + 8448 + lane * 4) = (lam.y * Lam.x - (lam.x - 1.0f) * Lam.y) * inv;
            waitv<0>(); waitl();
            STAMP(3);
            const int brow = cid * 32 + wave * 4 + (lane >> 4);
            u32x4 br[2][4];
#pragma unroll
            for (int c = 0; c < 8; ++c)
                br[c >> 2][c & 3] = *(const GAS u32x4*)((const GAS char*)(c < 4 ? p.bre : p.bim) + brow * 1024 + ((lane & 15) + 16 * (c & 3)) * 16);
            SCHED();
#pragma unroll
            for (int u = 0; u < 4; ++u) {
                const u32x4 cr = *(LAS u32x4*)(K + (rd0 ^ (32 * u))), ci = *(LAS u32x4*)(K + 4096 + (rd0 ^ (32 * u)));
                u32x4 o_re, o_im;
#pragma unroll
                for (int e = 0; e < 4; ++e) {
                    const f32x2 kx = *(LAS f32x2*)(K + 8192 + (u * 16 + 8 * hh + 2 * e) * 4), ky = *(LAS f32x2*)(K + 8448 + (u * 16 + 8 * hh + 2 * e) * 4);
                    const f32x2 a2 = {bfl(cr[e]), bfh(cr[e])}, b2 = {bfl(ci[e]), bfh(ci[e])};
                    const f32x2 re = a2 * kx - b2 * ky, im = -(a2 * ky) - b2 * kx;
                    o_re[e] = pk(re.x, re.y); o_im[e] = pk(im.x, im.y);
                }
                const int o = WS_CT + ((cid >> 1) * 64 + wave * 8 + (u >> 1) * 4 + (u & 1)) * 2048 + (cid & 1) * 1024;
                st128(o_re, rws, lane * 16, o, SC1);
                st128(o_im, rws, lane * 16, o + 4096, SC1);
            }
            STAMP(1);
            waitv<0>();
            if (lds_last(L + L_CNT + C_PUB2, 8, lane)) set_flag(rws, WS_FLC + cid * FLS, TLO, THI, lane);
            STAMP(2);
            float sm[2];
#pragma unroll
            for (int pt = 0; pt < 2; ++pt) {
                f32x2 s2 = {0.f, 0.f};
#pragma unroll
                for (int c = 0; c < 4; ++c)
#pragma unroll
                    for (int e = 0; e < 4; ++e) { const f32x2 t = {bfl(br[pt][c][e]), bfh(br[pt][c][e])}; s2 += t; }
                sm[pt] = row_sum(s2.x + s2.y);
            }
            if ((lane & 15) == 0) {
                u32x4 sv = {asu(sm[0]), TLO, asu(sm[1]), TLO};
                st128(sv, rws, brow * 32 + 16, WS_CH, SC1);
            }
            STAMP(7);
        }
    }

    if (wave < 4) {
        Walk ww(s0 * 16 * (S * 2) + tt * 128, 16 * S * 2, ns, rot);
        if (role == 0) {
            const int kq = xg >> 1, md = j & 3, ft = (xg & 1) * 6 + (j >> 2);
            const rsrc_t rb1 = mk(p.b1, F * 2);
            u32x2 bia[4];
#pragma unroll
            for (int g = 0; g < 4; ++g) bia[g] = __builtin_amdgcn_raw_buffer_load_b64(rb1, (ft * 128 + kq * 32 + 8 * g + 4 * hh) * 2, 0, 0);
            epi_load(ep, p, rws, tt, dt, tid);
            STAMP(11);
            waitv<0>();
            GET_TAG();
            STAMP(1);
            if (wave == 0) poll_flags(rws, WS_FLLN + (md * 128 + kq * 32 + l31) * FLS, TLO, THI);
            lds_add(L + L_CNT + C_GO, lane);
            lds_wait<0>(L + L_CNT + C_GO, 4);
            STAMP(2); CYC(cyc0);
            u32x4 hf[32], wf[4];
            const unsigned ho = WS_H2 + ((2 * md + (wave >> 1)) * 128 + kq * 32) * 2048 + (wave & 1) * 1024;
#pragma unroll
            for (int i = 0; i < 8; ++i) hf[i] = __builtin_amdgcn_raw_buffer_load_b128(rws, lane * 16, ho + i * 2048, SC1);
            LAS char* ta[4];
#pragma unroll
            for (int a = 0; a < 4; ++a) {
                ta[a] = L + (4 * hh + q4) * 256 + (4 * (a ^ kq ^ q4) + 2 * cb + (p4 >> 1)) * 16 + 8 * (p4 & 1);
                acc[a] = (f32x16)(0.f);
            }
            const unsigned t16 = TLO << 16;
            const int px = WS_PX + (md * 11 + ft) * 16 * 9216 + wave * 9216;
            u32x4 gx[9];
            auto send = [&](auto ai) __attribute__((always_inline)) {
                constexpr int a = decltype(ai)::value;
#pragma unroll
                for (int g = 0; g < 3; ++g) {
                    u32x4 o;
                    o.x = pk(acc[a][6 * g], acc[a][6 * g + 1]);
                    o.y = (pk(acc[a][6 * g + 2], 0.f) & 0xffffu) | t16;
                    o.z = g < 2 ? pk(acc[a][g < 2 ? 6 * g + 3 : 15], acc[a][g < 2 ? 6 * g + 4 : 15]) : pk(acc[a][15], 0.f);
                    o.w = (g < 2 ? (pk(acc[a][g < 2 ? 6 * g + 5 : 15], 0.f) & 0xffffu) : 0u) | t16;
                    st128(o, rws, lane * 16 + g * 1024, px + (a ^ kq) * 4 * 9216 + (a - 1) * 3072, SC1);
                }
            };
            auto fetch = [&](int k) __attribute__((always_inline)) { gx[k] = __builtin_amdgcn_raw_buffer_load_b128(rws, lane * 16 + (k & 3) * 1024, px + kq * 4 * 9216 + (k >> 2) * 4096, SC1); };
#pragma unroll
            for (int n = 0; n < 4; ++n) wf[n] = trread2(ta[tile_of(n)] + step_of(n) * 4096, ta[tile_of(n)] + step_of(n) * 4096 + 2048);
            SCHED();
            sfor<128>([&](auto ni) __attribute__((always_inline)) {
                constexpr int n = decltype(ni)::value;
                acc[tile_of(n)] = mfma(wf[n & 3], hf[step_of(n)], acc[tile_of(n)]);
                if constexpr (n + 4 < 128) wf[n & 3] = trread2(ta[tile_of(n + 4)] + step_of(n + 4) * 4096, ta[tile_of(n + 4)] + step_of(n + 4) * 4096 + 2048);
                if constexpr (n < 48 && (n & 1)) hf[8 + (n >> 1)] = __builtin_amdgcn_raw_buffer_load_b128(rws, lane * 16, ho + (8 + (n >> 1)) * 2048, SC1);
                if constexpr (n == NH2) lds_add(L + L_CNT + C_H2, lane);
                if constexpr (n == 65) send(std::integral_constant<int, 3>{});
                if constexpr (n == 67) send(std::integral_constant<int, 2>{});
                if constexpr (n == 97) send(std::integral_constant<int, 1>{});
                if constexpr (n == 116) { fetch(3); fetch(4); fetch(5); fetch(6); fetch(7); fetch(8); }
                SCHED();
            });
            STAMP(3); CYC(cyc1);
            lds_arrive(L + L_CNT + C_G3, lane);
            fetch(0); fetch(1); fetch(2);
#pragma unroll 1
            for (unsigned sp = 0; sp < SPIN_CAP; ++sp) {
                bool again = false;
#pragma unroll
                for (int s3 = 2; s3 >= 0; --s3) {
                    unsigned bad = 0;
#pragma unroll
                    for (int k = 3 * s3; k < 3 * s3 + 3; ++k) bad |= (gx[k].y ^ t16) | (gx[k].w ^ t16);
                    if (__builtin_amdgcn_ballot_w64(bad > 0xffffu) != 0) {
                        asm volatile("" ::: "memory");
                        fetch(3 * s3); fetch(3 * s3 + 1); fetch(3 * s3 + 2);
                        again = true;
                    }
                }
                if (!again) break;
            }
            STAMP(7);
#pragma unroll
            for (int k = 0; k < 9; ++k) {
                const int g = k % 3;
                acc[0][6 * g] += bfl(gx[k].x); acc[0][6 * g + 1] += bfh(gx[k].x); acc[0][6 * g + 2] += bfl(gx[k].y); acc[0][6 * g + 3] += bfl(gx[k].z);
                if (g < 2) { acc[0][6 * g + 4] += bfh(gx[k].z); acc[0][6 * g + 5] += bfl(gx[k].w); }
            }
            u32x4 uf[2];
#pragma unroll
            for (int g = 0; g < 4; ++g) {
                const float bs[4] = {bfl(bia[g].x), bfh(bia[g].x), bfl(bia[g].y), bfh(bia[g].y)};
                float o[4];
#pragma unroll
                for (int e = 0; e < 4; ++e) {
                    const float a = acc[0][4 * g + e] + bs[e];
                    const float z = a * fmaf(a * a, 0.044715f, 1.0f) * (-2.0f * 0.7978845608028654f * 1.4426950408889634f);
                    o[e] = a * __builtin_amdgcn_rcpf(1.0f + __builtin_amdgcn_exp2f(z));
                }
                uf[g >> 1][(g & 1) * 2] = pk(o[0], o[1]); uf[g >> 1][(g & 1) * 2 + 1] = pk(o[2], o[3]);
            }
            const int uo = WS_U + ((2 * md + (wave >> 1)) * 88 + ft * 8 + kq * 2) * 2048 + (wave & 1) * 1024;
            st128(uf[0], rws, lane * 16, uo, SC1);
            st128(uf[1], rws, lane * 16, uo + 2048, SC1);
            waitv<0>();
            if (lds_last(L + L_CNT + C_PUB, 4, lane)) {
                lds_wait<0>(L + L_CNT + C_SEEN, 8);
                set_flag(rws, WS_FLU + (md * 44 + ft * 4 + kq) * FLS, TLO, THI, lane);
            }
            STAMP(10);
            lds_wait<0>(L + L_CNT + C_RING, 4);
            ww.cur = ww.lo + (rot + RSL >= ns ? rot + RSL - ns : rot + RSL) * ww.step;
        } else {
            if (role == 1) { GET_TAG(); }
            else { const unsigned long long t_ = __builtin_amdgcn_s_memrealtime(); while (__builtin_amdgcn_s_memrealtime() - t_ < 200) __builtin_amdgcn_s_sleep(4); }
            epi_load(ep, p, rws, tt, dt, tid);
#pragma unroll 1
            for (int i = 0; i < RSL; ++i) { w_issue(ringaddr + i * 2048, w2v, rw2, ww); waitv<THR>(); }
            waitv<0>();
        }
        LAS char* ring = L + L_RING + wave * (RSL * 2048);
        const unsigned tr0 = (4 * hh + q4) * 128 + (4 * (q4 >> 1) + 2 * cb + (p4 >> 1)) * 16 + 8 * (p4 & 1), tr1 = tr0 ^ 64;
        u32x4 wp[RSL][2];
#pragma unroll
        for (int i = 0; i < RSL; ++i) {
            wp[i][0] = trread2(ring + i * 2048 + tr0, ring + i * 2048 + tr0 + 1024);
            wp[i][1] = trread2(ring + i * 2048 + tr1, ring + i * 2048 + tr1 + 1024);
        }
        waitl();
#pragma unroll 1
        for (int i = 0; i < RSL; ++i) { w_issue(ringaddr + i * 2048, w2v, rw2, ww); waitv<THR>(); }
        STAMP(4);
        {
            const int n0 = s0 >> 1, nf = ((s0 + ns - 1) >> 1) - n0 + 1;
            poll_flags(rws, WS_FLU + ((dt >> 1) * 44 + n0 + (lane < nf ? lane : 0)) * FLS, TLO, THI);
        }
        waitv<0>();
        STAMP(5);
        constexpr int DA = 6;
        u32x4 af[DA][2], wf[2][2];
        Walk aw(WS_U + (dt * 88 + s0) * 2048, 2048, ns, rot);
#pragma unroll
        for (int i = 0; i < DA; ++i) {
            const unsigned o = aw.next();
            af[i][0] = __builtin_amdgcn_raw_buffer_load_b128(rws, lane * 16, o, SC1);
            af[i][1] = __builtin_amdgcn_raw_buffer_load_b128(rws, lane * 16 + 1024, o, SC1);
        }
#pragma unroll
        for (int a = 0; a < 4; ++a) acc[a] = (f32x16)(0.f);
        SCHED();
#pragma unroll
        for (int i = 0; i < 22; ++i) {
            if (i + 1 < 22 && i + 1 >= RSL) {
                LAS char* sl = ring + (i + 1 - RSL) * 2048;
                wf[(i + 1) & 1][0] = trread2(sl + tr0, sl + tr0 + 1024);
                wf[(i + 1) & 1][1] = trread2(sl + tr1, sl + tr1 + 1024);
            }
            SCHED();
            if (i + 1 < 22 || wave < 2) {
#pragma unroll
                for (int a = 0; a < 4; ++a) acc[a] = mfma(af[i % DA][a >> 1], i < RSL ? wp[i < RSL ? i : 0][a & 1] : wf[i & 1][a & 1], acc[a]);
            }
            SCHED();
            if (i + DA < 22) {
                const unsigned o = aw.next();
                af[i % DA][0] = __builtin_amdgcn_raw_buffer_load_b128(rws, lane * 16, o, SC1);
                af[i % DA][1] = __builtin_amdgcn_raw_buffer_load_b128(rws, lane * 16 + 1024, o, SC1);
            }
            SCHED();
        }
        STAMP(6);
        if (b == 161 && wave == 0) {
#pragma unroll 1
            for (int i = 0; i < 4; ++i) poll_flags(rws, WS_FLU + (64 * i + lane) * FLS, TLO, THI);
            set_flag(rws, WS_EPOCH, TLO, THI, lane);
        }
    } else {
        __builtin_amdgcn_s_setprio(2);
        const int v = wave - 4, g = lane >> 4, li = lane & 15;
        LAS char* Q = L + L_AUX + v * 16384;
        if (j < 16) {
            const int lg = xg * 16 + j;
            f32x2 f[16];
#pragma unroll
            for (int c = 0; c < 4; ++c)
#pragma unroll
                for (int e = 0; e < 4; ++e) { f[4 * c + e].x = bfl(xr[c][e]); f[4 * c + e].y = bfh(xr[c][e]); }
            STAMP(7);
            f32x2 s2 = f[0];
#pragma unroll
            for (int e = 1; e < 16; ++e) s2 += f[e];
            const float m = row_sum(s2.x + s2.y) * (1.0f / D);
            f32x2 q2 = {0.f, 0.f};
#pragma unroll
            for (int e = 0; e < 16; ++e) { f[e] -= m; q2 += f[e] * f[e]; }
            const float var = row_sum(q2.x + q2.y) * (1.0f / D);
            const float ra = __builtin_amdgcn_rsqf(var + EPS);
            const float sc = ra * __builtin_amdgcn_rsqf(var * ra * ra + EPS);
#pragma unroll
            for (int c = 0; c < 4; ++c) {
                u32x4 o;
#pragma unroll
                for (int e = 0; e < 4; ++e) { const f32x2 t = f[4 * c + e] * sc; o[e] = pk(t.x, t.y); }
                *(LAS u32x4*)(L + L_LN + (4 * v + g) * 1024 + (((li + 16 * c) ^ (g << 2)) * 16)) = o;
            }
            lds_arrive(L + L_CNT + C_LN, lane);
            GET_TAG();
            if (li == 0) {
                u32x4 sv = {asu(m), TLO, asu(ra), TLO};
                st128(sv, rws, g * 16, WS_STATS + (lg * 16 + 4 * v) * 16, SC1);
            }
            STAMP(10);
            lds_wait<0>(L + L_CNT + C_LN, 4);
            STAMP(4);
#pragma unroll
            for (int i = 0; i < 4; ++i) {
                LAS char* a = L + L_LN + (4 * hh + q4) * 1024 + (16 * v + 4 * (i ^ q4) + 2 * cb + (p4 >> 1)) * 16 + 8 * (p4 & 1);
                const u32x4 fr = trread2(a, a + 8192);
                st128(fr, rws, lane * 16, WS_H2 + ((2 * v + (i >> 1)) * 128 + lg) * 2048 + (i & 1) * 1024, SC1);
            }
            STAMP(1);
            waitv<0>();
            set_flag(rws, WS_FLLN + (v * 128 + lg) * FLS, TLO, THI, lane);
            STAMP(2);
        } else if (j < 24) {
            if (role == 0) { GET_TAG(); }
        } else {
            const int ch = pg * 128 + v * 32 + l31;
            unsigned rd[4];
#pragma unroll
            for (int ks = 0; ks < 4; ++ks) rd[ks] = rd0 ^ (32 * ks);

            f32x16 ac[4][2];
#pragma unroll
            for (int a = 0; a < 8; ++a) { ac[a >> 1][a & 1] = (f32x16)(0.f); asm volatile("" : "+v"(ac[a >> 1][a & 1])); }
            u32x4 fr[2][6];
            const unsigned bo = 16384 + v * 8192;
            u32x4 sg0, sg1, cg, cl;
            STAMP(13);
            bar();
            STAMP(1); CYC(cyc0);
#pragma unroll
            for (int k = 0; k < 6; ++k) fr[0][k] = *(LAS u32x4*)(L + rd[0] + (k < 2 ? bo + k * 4096 : (k - 2) * 4096));
            unsigned buf = 0;
#pragma unroll 1
            for (int n = 0; n < 8; ++n) {
#pragma unroll
                for (int ks = 0; ks < 4; ++ks) {
                    if (ks == 3) { bar(); buf = buf == 98304 ? 0 : buf + 49152; }
                    LAS char* A = L + buf + rd[(ks + 1) & 3];
                    LAS char* B = A + bo;
                    SCHED();
#pragma unroll
                    for (int k = 0; k < 8; ++k) {
                        ac[k >> 1][k & 1] = mfma(fr[ks & 1][2 + (k >> 1)], fr[ks & 1][k & 1], ac[k >> 1][k & 1]);
                        if (k < 6) fr[(ks + 1) & 1][k] = *(LAS u32x4*)(k < 2 ? B + k * 4096 : A + (k - 2) * 4096);
                        SCHED();
                    }
                }
                if (n == 6) {
                    GET_TAG();
                    epi_load(ep, p, rws, tt, dt, tid);
                    sg0 = __builtin_amdgcn_raw_buffer_load_b128(rws, lane * 16, WS_STATS + ct * 2048, SC1);
                    sg1 = __builtin_amdgcn_raw_buffer_load_b128(rws, lane * 16 + 1024, WS_STATS + ct * 2048, SC1);
                    cl = __builtin_amdgcn_raw_buffer_load_b128(rws, ch * 32, WS_CH, SC1); cg = __builtin_amdgcn_raw_buffer_load_b128(rws, ch * 32 + 16, WS_CH, SC1);
                }
            }
            STAMP(2); CYC(cyc1);
#pragma unroll 1
            for (unsigned sp = 0; sp < SPIN_CAP; ++sp) {
                const unsigned bad = (sg0.y ^ tlo) | (sg0.w ^ tlo) | (sg1.y ^ tlo) | (sg1.w ^ tlo) | (cg.y ^ tlo) | (cg.w ^ tlo) | (cl.y ^ tlo) | (cl.w ^ tlo);
                if (__builtin_amdgcn_ballot_w64(bad != 0) == 0) break;
                __builtin_amdgcn_s_sleep(1);
                asm volatile("" ::: "memory");
                sg0 = __builtin_amdgcn_raw_buffer_load_b128(rws, lane * 16, WS_STATS + ct * 2048, SC1);
                sg1 = __builtin_amdgcn_raw_buffer_load_b128(rws, lane * 16 + 1024, WS_STATS + ct * 2048, SC1);
                cl = __builtin_amdgcn_raw_buffer_load_b128(rws, ch * 32, WS_CH, SC1); cg = __builtin_amdgcn_raw_buffer_load_b128(rws, ch * 32 + 16, WS_CH, SC1);
            }
            {
                LAS float* T = (LAS float*)(Q + l31 * 16 + hh * 4);
                T[0] = asf(sg0.z); T[2] = -asf(sg0.x) * asf(sg0.z);
                T[128] = asf(sg1.z); T[130] = -asf(sg1.x) * asf(sg1.z);
            }
            waitl();
            const float sbr = asf(cg.x), sbi = asf(cg.z);
            const f32x2 lam = {asf(cl.x), asf(cl.z)};
            f32x2 er = {0.f, 0.f}, ei = {0.f, 0.f};
            f32x4 ab[2][8];
#pragma unroll
            for (int i = 0; i < 8; ++i) ab[0][i] = *(LAS f32x4*)(Q + hh * 512 + i * 16);
#pragma unroll
            for (int m = 0; m < 4; ++m) {
                if (m < 3) {
#pragma unroll
                    for (int i = 0; i < 8; ++i) ab[(m + 1) & 1][i] = *(LAS f32x4*)(Q + hh * 512 + (8 * m + 8 + i) * 16);
                }
                SCHED();
#pragma unroll
                for (int i = 0; i < 8; ++i) {
                    const f32x2 a2 = {ab[m & 1][i].x, ab[m & 1][i].y}, b2 = {ab[m & 1][i].z, ab[m & 1][i].w};
                    const int r = 2 * i;
                    f32x2 vr = {ac[m][0][r], ac[m][0][r + 1]}, vi = {ac[m][1][r], ac[m][1][r + 1]};
                    vr = a2 * vr + b2 * sbr; vi = a2 * vi + b2 * sbi;
                    ac[m][0][r] = vr.x; ac[m][0][r + 1] = vr.y; ac[m][1][r] = vi.x; ac[m][1][r + 1] = vi.y;
                    const f32x2 nr = lam.x * er + (vr - lam.y * ei), ni = lam.x * ei + (vi + lam.y * er);
                    er = nr; ei = ni;
                }
                SCHED();
            }
            f32x2 l32 = lam;
#pragma unroll
            for (int i = 0; i < 5; ++i) l32 = cmul(l32, l32);
            const f32x2 l64 = cmul(l32, l32), l128 = cmul(l64, l64), l256 = cmul(l128, l128);
            const f32x2 e0 = {er.x, ei.x}, e1 = {er.y, ei.y};
            const f32x2 eh = cfma(l32, e0, e1);
            const f32x2 eo = {other_half(eh.x), other_half(eh.y)};
            if (hh == 0) {
                const f32x2 E = cfma(l64, eh, eo);
                u32x4 ge = {asu(E.x), tlo, asu(E.y), tlo};
                st128(ge, rws, ch * 16, WS_E + ct * (P * 16), SC1);
            }
            STAMP(3);
            f32x2 H = {0.f, 0.f};
            {
                u32x4 ge[8];
#pragma unroll 1
                for (unsigned sp = 0; sp < SPIN_CAP; ++sp) {
                    asm volatile("" ::: "memory");
                    bool bad = false;
#pragma unroll
                    for (int qq = 0; qq < 8; ++qq) {
                        const int jc = ct - 1 - hh - 2 * qq;
                        ge[qq] = __builtin_amdgcn_raw_buffer_load_b128(rws, jc < 0 ? 0x40000000 : jc * (P * 16) + ch * 16, WS_E, SC1);
                        bad |= jc >= 0 && (ge[qq].y != tlo || ge[qq].w != tlo);
                    }
                    if (__builtin_amdgcn_ballot_w64(bad) == 0) break;
                }
#pragma unroll
                for (int qq = 7; qq >= 0; --qq) { const f32x2 ev = {asf(ge[qq].x), asf(ge[qq].z)}; H = cfma(l256, H, ev); }
                if (hh) H = cmul(H, l128);
                H.x += other_half(H.x); H.y += other_half(H.y);
            }
            STAMP(4);
            if (hh) H = cfma(l64, H, eo);
            const f32x2 H1 = cfma(l32, H, e0);
            f32x2 hr = {H.x, H1.x}, hi = {H.y, H1.y};
            const int fw = ((l31 & 3) << 2) | ((l31 >> 1) & 3);
#pragma unroll
            for (int m = 0; m < 4; ++m) {
                u32x4 o[4];
#pragma unroll
                for (int i2 = 0; i2 < 4; ++i2) {
                    const int r = 4 * i2;
                    const f32x2 vr0 = {ac[m][0][r], ac[m][0][r + 1]}, vi0 = {ac[m][1][r], ac[m][1][r + 1]};
                    const f32x2 vr1 = {ac[m][0][r + 2], ac[m][0][r + 3]}, vi1 = {ac[m][1][r + 2], ac[m][1][r + 3]};
                    const f32x2 ar = lam.x * hr + (vr0 - lam.y * hi), ai = lam.x * hi + (vi0 + lam.y * hr);
                    hr = lam.x * ar + (vr1 - lam.y * ai); hi = lam.x * ai + (vi1 + lam.y * ar);
                    o[0][i2] = pk(ar.x, hr.x); o[1][i2] = pk(ar.y, hr.y); o[2][i2] = pk(ai.x, hi.x); o[3][i2] = pk(ai.y, hi.y);
                }
#pragma unroll
                for (int u = 0; u < 4; ++u)
                    *(LAS u32x4*)(Q + (u >> 1) * 8192 + l31 * 256 + (((8 * hh + 4 * (u & 1) + m) ^ fw) * 16)) = o[u];
            }
            waitl();
            {
                const int r0 = 8 * hh + q4, f0 = (q4 << 2) | ((r0 >> 1) & 3), f1 = (q4 << 2) | (((r0 + 4) >> 1) & 3);
                const int ho = WS_H + (2 * ct * 64 + pg * 16 + v * 4) * 2048;
                u32x4 fo[16];
#pragma unroll
                for (int tq = 0; tq < 4; ++tq) {
                    const int c = 4 * tq + 2 * cb + (p4 >> 1);
                    LAS char* a0 = Q + r0 * 256 + ((c ^ f0) * 16) + 8 * (p4 & 1);
                    LAS char* a1 = Q + (r0 + 4) * 256 + ((c ^ f1) * 16) + 8 * (p4 & 1);
#pragma unroll
                    for (int kk = 0; kk < 4; ++kk) fo[tq * 4 + kk] = trread2(a0 + kk * 4096, a1 + kk * 4096);
                }
                SCHED();
#pragma unroll
                for (int k = 0; k < 16; ++k)
                    st128(fo[k], rws, lane * 16 + ((k >> 2) & 1) * 1024, ho + ((k >> 3) * 64 + (k & 3)) * 2048, SC1);
            }
            STAMP(7);
            waitv<0>();
            if (lds_last(L + L_CNT + C_PUB, 4, lane)) {
                lds_wait<0>(L + L_CNT + C_SEEN, 8);
                set_flag(rws, WS_FLH + (ct * 4 + pg) * FLS, tlo, THI, lane);
            }
            STAMP(10);
        }
        constexpr int CP = 14, HD = 5;
        if (role != 1) {
            __builtin_amdgcn_s_setprio(0);
            if (role == 0) lds_wait<2>(L + L_CNT + C_SEEN, 8);
            epi_load(ep, p, rws, tt, dt, tid);
        }
        if (role == 0) {
            lds_wait<2>(L + L_CNT + C_H2, 4);
            const unsigned pv = hh * (S * 2) + l31 * 16;
#pragma unroll 1
            for (int i = 0; i < 9; ++i) {
                const int pc = (i * 20 + j) * 4 + v;
                if (j < 20 && pc < 688) glds<W2NT>(Lb + L_DUMMY + v * 1024, pv, rw2, pc * 2 * (S * 2) + xg * 512);
                waitv<THR>();
            }
            STAMP(13);
        }
        poll_flags(rws, WS_FLC + (2 * dt + (lane & 1)) * FLS, TLO, THI);
        u32x4 cf[CP][2], hf[HD][2];
        const unsigned co = WS_CT + (dt * 64 + v * 16) * 2048, ho = WS_H + (tt * 64 + v * 16) * 2048;
#pragma unroll
        for (int i = 0; i < CP; ++i) {
            if (i == CP1 && role == 0) {
                STAMP(14);
                Walk ww(s0 * 16 * (S * 2) + tt * 128, 16 * S * 2, ns, rot);
                lds_wait<2>(L + L_CNT + C_G3, 4);
#pragma unroll 1
                for (int k = 0; k < RSL; ++k) { w_issue(ringaddr + k * 2048, w2v, rw2, ww); waitv<THR>(); }
                waitv<0>();
                lds_add(L + L_CNT + C_RING, lane);
                STAMP(3);
                lds_wait<1>(L + L_CNT + C_PUB, 4);
            }
            cf[i][0] = __builtin_amdgcn_raw_buffer_load_b128(rws, lane * 16, co + i * 2048, SC1);
            cf[i][1] = __builtin_amdgcn_raw_buffer_load_b128(rws, lane * 16 + 1024, co + i * 2048, SC1);
            waitv<THR>();
        }
        SCHED();
        STAMP(11);
        poll_flags(rws, WS_FLH + ((tt >> 1) * 4 + v) * FLS, TLO, THI);
        STAMP(5);
#pragma unroll
        for (int i = 0; i < HD; ++i) {
            hf[i][0] = __builtin_amdgcn_raw_buffer_load_b128(rws, lane * 16, ho + i * 2048, SC1);
            hf[i][1] = __builtin_amdgcn_raw_buffer_load_b128(rws, lane * 16 + 1024, ho + i * 2048, SC1);
        }
#pragma unroll
        for (int a = 0; a < 4; ++a) acc[a] = (f32x16)(0.f);
        SCHED();
#pragma unroll
        for (int i = 0; i < 16; ++i) {
#pragma unroll
            for (int a = 0; a < 4; ++a) acc[a] = mfma(cf[i % CP][a >> 1], hf[i % HD][a & 1], acc[a]);
            SCHED();
            if (i + CP < 16) {
                cf[i % CP][0] = __builtin_amdgcn_raw_buffer_load_b128(rws, lane * 16, co + (i + CP) * 2048, SC1);
                cf[i % CP][1] = __builtin_amdgcn_raw_buffer_load_b128(rws, lane * 16 + 1024, co + (i + CP) * 2048, SC1);
            }
            if (i + HD < 16) {
                hf[i % HD][0] = __builtin_amdgcn_raw_buffer_load_b128(rws, lane * 16, ho + (i + HD) * 2048, SC1);
                hf[i % HD][1] = __builtin_amdgcn_raw_buffer_load_b128(rws, lane * 16 + 1024, ho + (i + HD) * 2048, SC1);
            }
            SCHED();
        }
        STAMP(6);
        if (role == 0) lds_wait<0>(L + L_CNT + C_RING, 4);
    }
    {
        LAS char* I = L + (wave < 4 ? L_RING + wave * (RSL * 2048) : L_AUX + (wave - 4) * 16384);
        const int sw = (l31 & 7) ^ (((l31 >> 1) & 1) << 3);
#pragma unroll
        for (int a = 0; a < 4; ++a)
#pragma unroll
            for (int g = 0; g < 4; ++g) {
                f32x4 v = {acc[a][4 * g], acc[a][4 * g + 1], acc[a][4 * g + 2], acc[a][4 * g + 3]};
                *(LAS f32x4*)(I + ((a & 1) * 32 + l31) * 256 + ((((a >> 1) * 8 + 2 * g + hh) ^ sw) * 16)) = v;
            }
    }
    bar();
    STAMP(8);
    {
        const int t = tid >> 3, sw = (t & 7) ^ (((t >> 1) & 1) << 3);
        const int et = tt * 64 + t, ed = dt * 64 + (tid & 7) * 4;
        if (ep.sg.y != tlo || ep.sg.w != tlo) ep.sg = __builtin_amdgcn_raw_buffer_load_b128(rws, et * 16, WS_STATS, SC1);
        const float mean = asf(ep.sg.x), rstd = asf(ep.sg.z);
#pragma unroll
        for (int h2 = 0; h2 < 2; ++h2) {
            LAS char* R = L + t * 256 + ((((tid & 7) + 8 * h2) ^ sw) * 16);
            f32x4 y2 = *(LAS f32x4*)(R + L_RING), y = *(LAS f32x4*)(R + L_AUX);
#pragma unroll
            for (int w = 1; w < 4; ++w) { y2 += *(LAS f32x4*)(R + L_RING + w * (RSL * 2048)); y += *(LAS f32x4*)(R + L_AUX + w * 16384); }
            const u32x2 xx = h2 ? ep.x1 : ep.x0, kk = h2 ? ep.k1 : ep.k0;
            const float xf[4] = {bfl(xx.x), bfh(xx.x), bfl(xx.y), bfh(xx.y)};
            const float kf[4] = {bfl(kk.x), bfh(kk.x), bfl(kk.y), bfh(kk.y)};
            f32x4 o;
#pragma unroll
            for (int e = 0; e < 4; ++e) {
                const float z = y2[e] + ep.b2;
                const float g = z * __builtin_amdgcn_rcpf(1.0f + __builtin_amdgcn_exp2f(z * -1.4426950408889634f));
                const float s = fmaf((xf[e] - mean) * rstd, kf[e], y[e]);
                o[e] = fmaf(s, g, xf[e]);
            }
            st128(__builtin_bit_cast(u32x4, o), mk(p.out, S * D * 4), (et * D + ed + h2 * 32) * 4, 0, SC1);
        }
    }
    STAMP(9);
}

extern "C" void kernel_launch(void* const* d_in, const int* in_sizes, int n_in, void* d_out, int out_size, void* d_ws, size_t ws_size,
                              hipStream_t stream) {
    Params p;
    p.x = d_in[0]; p.lre = d_in[1]; p.lim = d_in[2]; p.bre = d_in[3]; p.bim = d_in[4]; p.cre = d_in[5]; p.cim = d_in[6];
    p.ldt = d_in[7]; p.dsk = d_in[8]; p.w1 = d_in[10]; p.b1 = d_in[11]; p.w2 = d_in[12]; p.b2 = d_in[13];
#define WSOFF 0
    p.out = d_out; p.ws = (char*)d_ws + (size_t)WSOFF * 1048576;
    fused_kernel<<<256, 512, 0, stream>>>(p);
}
```
